```python
import torch

def _softmax(x, axis=-1):
    return torch.softmax(x, dim=axis)
def _gelu(x):
    return torch.nn.functional.gelu(x, approximate="tanh")
def _silu(x):
    return torch.nn.functional.silu(x)
def _sigmoid(x):
    return torch.sigmoid(x)
def _layernorm(x, axis=-1, eps=1e-5):
    m = x.mean(dim=axis, keepdim=True)
    v = x.var(dim=axis, keepdim=True, unbiased=False)
    return (x - m) / torch.sqrt(v + eps)
def _scatter_add(tgt, idx, src, axis=0):
    return tgt.clone().index_add_(axis, idx, src)
def _segment_sum(src, seg, n):
    out = torch.zeros((n,) + src.shape[1:], dtype=src.dtype)
    return out.index_add_(0, seg, src)
def _pad(x, pw):
    flat = tuple(v for pair in reversed(pw) for v in pair)
    return torch.nn.functional.pad(x, flat)

kernel_name = 'standalone__molecular_angular_gemnet__graph'

def setup_inputs():

    _w = (lambda a: (lambda t: t.to(torch.bfloat16) if t.is_floating_point() else t)(torch.as_tensor(a)))
    N, deg, De, L = 12500, 32, 4, 1
    E = N * deg
    T = E * deg
    NR = 8
    dist = torch.rand(E).to(float)
    angle = (torch.rand(T) * torch.pi).to(float)
    centers = torch.linspace(0.0, 1.0, NR).to(float)
    ang_freq = torch.arange(1, L + 1).to(float)
    return {
        "m": _w((lambda *s: torch.randn(*s, dtype=torch.bfloat16) * 0.05)(E, De)),
        "idx_in": _w((lambda lo, hi, *s: torch.randint(lo, hi, s))(0, E, T).to(torch.int64)),
        "idx_out": _w((lambda lo, hi, *s: torch.randint(lo, hi, s))(0, E, T).to(torch.int64)),
        "dist": _w(dist),
        "angle": _w(angle),
        "centers": _w(centers),
        "ang_freq": _w(ang_freq),
        "w_rbf": _w((lambda *s: torch.randn(*s, dtype=torch.bfloat16) * 0.4)(NR, De)),
        "w_sbf": _w((lambda *s: torch.randn(*s, dtype=torch.bfloat16) * 0.4)(NR * L, De)),
        "w_down": _w((lambda *s: torch.randn(*s, dtype=torch.bfloat16) * 0.4)(De, De)),
        "w_up": _w((lambda *s: torch.randn(*s, dtype=torch.bfloat16) * 0.4)(De, De)),
    }

def reference(m, idx_in, idx_out, dist, angle, centers, ang_freq, w_rbf, w_sbf, w_down, w_up):
    E, De = m.shape[0], m.shape[1]
    NR = centers.shape[0]
    L = ang_freq.shape[0]
    rbf = torch.exp(-10.0 * (dist[:, None] - centers[None, :]) ** 2)
    cbf = torch.cos(ang_freq[None, :] * angle[:, None])
    rbf_t = rbf[idx_out]
    sbf = torch.reshape(rbf_t[:, :, None] * cbf[:, None, :], (-1, NR * L))
    x = (m[idx_in] @ w_down) * (sbf @ w_sbf)
    agg = _segment_sum(x, idx_out, E)
    return m + (agg @ w_up) * (rbf @ w_rbf)
```

```cpp
#include <hip/hip_runtime.h>
#include <hip/hip_bf16.h>
#include <stdint.h>

typedef unsigned short u16;
__device__ __forceinline__ float bf2f(u16 v) { return __uint_as_float(((unsigned)v) << 16); }
__device__ __forceinline__ float rbf16(float f) {
    __hip_bfloat16 b = __float2bfloat16(f);
    return __bfloat162float(b);
}

__global__ void zero_k(float4* acc, int n) {
    int i = blockIdx.x * blockDim.x + threadIdx.x;
    if (i < n) acc[i] = make_float4(0.f, 0.f, 0.f, 0.f);
}

__global__ void scat_k(const uint2* __restrict__ m, const int* __restrict__ idx_in, const int* __restrict__ idx_out,
                       const u16* __restrict__ angle, const u16* __restrict__ ang_freq, float* acc, int T) {
    int lane4 = threadIdx.x & 3;
    long long gid = (long long)blockIdx.x * blockDim.x + threadIdx.x;
    int t = (int)(gid >> 2);
    if (t >= T) return;
    float f = bf2f(ang_freq[0]);
    float a = rbf16(f * bf2f(angle[t]));
    float c = rbf16(cosf(a));
    int ii = idx_in[t], io = idx_out[t];
    const u16* mp = (const u16*)(m + ii);
    float v = bf2f(mp[lane4]);
    atomicAdd(&acc[(size_t)io * 4 + lane4], c * v);
}

__global__ void fin_k(const uint2* __restrict__ m, const u16* __restrict__ dist, const u16* __restrict__ centers,
                      const u16* __restrict__ w_rbf, const u16* __restrict__ w_sbf, const u16* __restrict__ w_down,
                      const u16* __restrict__ w_up, const float4* __restrict__ acc, uint2* out, int E) {
    int e = blockIdx.x * blockDim.x + threadIdx.x;
    if (e >= E) return;
    float d = bf2f(dist[e]);
    float g[4] = {0, 0, 0, 0}, h[4] = {0, 0, 0, 0};
    for (int r = 0; r < 8; r++) {
        float df = rbf16(d - bf2f(centers[r]));
        float sq = rbf16(df * df);
        float ar = rbf16(-10.0f * sq);
        float rb = rbf16(expf(ar));
        for (int c = 0; c < 4; c++) {
            g[c] += rb * bf2f(w_sbf[r * 4 + c]);
            h[c] += rb * bf2f(w_rbf[r * 4 + c]);
        }
    }
    float4 s4 = acc[e];
    float s[4] = {s4.x, s4.y, s4.z, s4.w};
    float agg[4];
    for (int c = 0; c < 4; c++) {
        float v = 0;
        for (int k = 0; k < 4; k++) v += s[k] * bf2f(w_down[k * 4 + c]);
        agg[c] = rbf16(v * g[c]);
    }
    uint2 mv = m[e];
    float mm[4] = {__uint_as_float(mv.x << 16), __uint_as_float(mv.x & 0xffff0000u), __uint_as_float(mv.y << 16), __uint_as_float(mv.y & 0xffff0000u)};
    u16 o[4];
    for (int c = 0; c < 4; c++) {
        float v = 0;
        for (int k = 0; k < 4; k++) v += agg[k] * bf2f(w_up[k * 4 + c]);
        float r = rbf16(rbf16(v) * rbf16(h[c]));
        __hip_bfloat16 b = __float2bfloat16(mm[c] + r);
        o[c] = *(u16*)&b;
    }
    out[e] = make_uint2(o[0] | ((unsigned)o[1] << 16), o[2] | ((unsigned)o[3] << 16));
}

extern "C" void kernel_launch(void* const* d_in, const int* in_sizes, int n_in,
                              void* d_out, int out_size, void* d_ws, size_t ws_size,
                              hipStream_t stream) {
    const uint2* m = (const uint2*)d_in[0];
    const int* idx_in = (const int*)d_in[1];
    const int* idx_out = (const int*)d_in[2];
    const u16* dist = (const u16*)d_in[3];
    const u16* angle = (const u16*)d_in[4];
    const u16* centers = (const u16*)d_in[5];
    const u16* ang_freq = (const u16*)d_in[6];
    const u16* w_rbf = (const u16*)d_in[7];
    const u16* w_sbf = (const u16*)d_in[8];
    const u16* w_down = (const u16*)d_in[9];
    const u16* w_up = (const u16*)d_in[10];
    int E = in_sizes[3];
    int T = in_sizes[1];
    float* acc = (float*)d_ws;
    zero_k<<<(E + 255) / 256, 256, 0, stream>>>((float4*)acc, E);
    long long nthr = (long long)T * 4;
    scat_k<<<(unsigned)((nthr + 255) / 256), 256, 0, stream>>>(m, idx_in, idx_out, angle, ang_freq, acc, T);
    fin_k<<<(E + 255) / 256, 256, 0, stream>>>(m, dist, centers, w_rbf, w_sbf, w_down, w_up, (const float4*)acc, (uint2*)d_out, E);
}
```

```cpp
#include <hip/hip_runtime.h>
#include <hip/hip_bf16.h>
#include <stdint.h>

typedef unsigned short u16;
__device__ __forceinline__ float bf2f(u16 v) { return __uint_as_float(((unsigned)v) << 16); }
__device__ __forceinline__ float rbf16(float f) {
    __hip_bfloat16 b = __float2bfloat16(f);
    return __bfloat162float(b);
}

__global__ void zero_k(float4* acc, int n) {
    int i = blockIdx.x * blockDim.x + threadIdx.x;
    if (i < n) acc[i] = make_float4(0.f, 0.f, 0.f, 0.f);
}

__global__ void scat_k(const uint2* __restrict__ m, const int* __restrict__ idx_in, const int* __restrict__ idx_out,
                       const u16* __restrict__ angle, const u16* __restrict__ ang_freq, float* acc, int T) {
    int lane4 = threadIdx.x & 3;
    long long gid = (long long)blockIdx.x * blockDim.x + threadIdx.x;
    int t = (int)(gid >> 2);
    if (t >= T) return;
    float f = bf2f(ang_freq[0]);
    float a = rbf16(f * bf2f(angle[t]));
    float c = rbf16(cosf(a));
    int ii = idx_in[t], io = idx_out[t];
    const u16* mp = (const u16*)(m + ii);
    float v = bf2f(mp[lane4]);
    atomicAdd(&acc[(size_t)io * 4 + lane4], c * v);
}

__global__ void fin_k(const uint2* __restrict__ m, const u16* __restrict__ dist, const u16* __restrict__ centers,
                      const u16* __restrict__ w_rbf, const u16* __restrict__ w_sbf, const u16* __restrict__ w_down,
                      const u16* __restrict__ w_up, const float4* __restrict__ acc, uint2* out, int E) {
    int e = blockIdx.x * blockDim.x + threadIdx.x;
    if (e >= E) return;
    float d = bf2f(dist[e]);
    float g[4] = {0, 0, 0, 0}, h[4] = {0, 0, 0, 0};
    for (int r = 0; r < 8; r++) {
        float df = rbf16(d - bf2f(centers[r]));
        float sq = rbf16(df * df);
        float ar = rbf16(-10.0f * sq);
        float rb = rbf16(expf(ar));
        for (int c = 0; c < 4; c++) {
            g[c] += rb * bf2f(w_sbf[r * 4 + c]);
            h[c] += rb * bf2f(w_rbf[r * 4 + c]);
        }
    }
    float4 s4 = acc[e];
    float s[4] = {s4.x, s4.y, s4.z, s4.w};
    float agg[4];
    for (int c = 0; c < 4; c++) {
        float v = 0;
        for (int k = 0; k < 4; k++) v += s[k] * bf2f(w_down[k * 4 + c]);
        agg[c] = rbf16(v * g[c]);
    }
    uint2 mv = m[e];
    float mm[4] = {__uint_as_float(mv.x << 16), __uint_as_float(mv.x & 0xffff0000u), __uint_as_float(mv.y << 16), __uint_as_float(mv.y & 0xffff0000u)};
    u16 o[4];
    for (int c = 0; c < 4; c++) {
        float v = 0;
        for (int k = 0; k < 4; k++) v += agg[k] * bf2f(w_up[k * 4 + c]);
        float r = rbf16(rbf16(v) * rbf16(h[c]));
        __hip_bfloat16 b = __float2bfloat16(mm[c] + r);
        o[c] = *(u16*)&b;
    }
    out[e] = make_uint2(o[0] | ((unsigned)o[1] << 16), o[2] | ((unsigned)o[3] << 16));
}


typedef unsigned int u32;
typedef u32 u32x4 __attribute__((ext_vector_type(4)));
template <int NT>
__global__ void __launch_bounds__(256) rd_k(const u32x4* __restrict__ p, size_t n16, u32* sink) {
    size_t i = (size_t)blockIdx.x * 256 + threadIdx.x;
    size_t stride = (size_t)gridDim.x * 256;
    u32 acc = 0;
    for (; i + 3 * stride < n16; i += 4 * stride) {
        u32x4 a, b, c, d;
        if (NT) { a = __builtin_nontemporal_load(p + i); b = __builtin_nontemporal_load(p + i + stride); c = __builtin_nontemporal_load(p + i + 2 * stride); d = __builtin_nontemporal_load(p + i + 3 * stride); }
        else { a = p[i]; b = p[i + stride]; c = p[i + 2 * stride]; d = p[i + 3 * stride]; }
        acc += a.x ^ a.y ^ a.z ^ a.w ^ b.x ^ b.y ^ b.z ^ b.w ^ c.x ^ c.y ^ c.z ^ c.w ^ d.x ^ d.y ^ d.z ^ d.w;
    }
    for (; i < n16; i += stride) { u32x4 a = p[i]; acc += a.x ^ a.y ^ a.z ^ a.w; }
    if (acc == 0x12345678u) sink[0] = acc;
}
template <int NT, int WR>
__global__ void __launch_bounds__(256) rw_k(const u32x4* __restrict__ p0, const u32x4* __restrict__ p1, const u32x4* __restrict__ p2, u32x4* __restrict__ q, size_t ngroups, u32* sink) {
    size_t i = (size_t)blockIdx.x * 256 + threadIdx.x;
    size_t stride = (size_t)gridDim.x * 256;
    u32 acc = 0;
    for (; i < ngroups; i += stride) {
        u32x4 a, b, c, d, e;
        if (NT & 1) { a = __builtin_nontemporal_load(p0 + i); b = __builtin_nontemporal_load(p0 + i + ngroups); c = __builtin_nontemporal_load(p1 + i); d = __builtin_nontemporal_load(p1 + i + ngroups); e = __builtin_nontemporal_load(p2 + i); }
        else { a = p0[i]; b = p0[i + ngroups]; c = p1[i]; d = p1[i + ngroups]; e = p2[i]; }
        u32x4 x = a ^ b, y = c ^ d, z = e ^ a;
        if (WR) {
        if (NT & 2) { __builtin_nontemporal_store(x, q + i); __builtin_nontemporal_store(y, q + i + ngroups); __builtin_nontemporal_store(z, q + i + 2 * ngroups); }
        else { q[i] = x; q[i + ngroups] = y; q[i + 2 * ngroups] = z; }
        } else { u32x4 w = x ^ y ^ z; acc += w.x ^ w.y ^ w.z ^ w.w; }
    }
    if (acc == 0x12345678u) sink[0] = acc;
}
extern "C" void kernel_launch(void* const* d_in, const int* in_sizes, int n_in,
                              void* d_out, int out_size, void* d_ws, size_t ws_size,
                              hipStream_t stream) {
    const uint2* m = (const uint2*)d_in[0];
    const int* idx_in = (const int*)d_in[1];
    const int* idx_out = (const int*)d_in[2];
    const u16* dist = (const u16*)d_in[3];
    const u16* angle = (const u16*)d_in[4];
    const u16* centers = (const u16*)d_in[5];
    const u16* ang_freq = (const u16*)d_in[6];
    const u16* w_rbf = (const u16*)d_in[7];
    const u16* w_sbf = (const u16*)d_in[8];
    const u16* w_down = (const u16*)d_in[9];
    const u16* w_up = (const u16*)d_in[10];
    int E = in_sizes[3];
    int T = in_sizes[1];
    float* acc = (float*)d_ws;
    const u32x4* P0 = (const u32x4*)idx_in; const u32x4* P1 = (const u32x4*)idx_out; const u32x4* P2 = (const u32x4*)angle;
    u32x4* Q = (u32x4*)((char*)d_ws + (16u << 20)); size_t NG = (size_t)T / 8; u32* SINK = (u32*)((char*)d_ws + (12u << 20));

    zero_k<<<(E + 255) / 256, 256, 0, stream>>>((float4*)acc, E);
    long long nthr = (long long)T * 4;
    scat_k<<<(unsigned)((nthr + 255) / 256), 256, 0, stream>>>(m, idx_in, idx_out, angle, ang_freq, acc, T);
    fin_k<<<(E + 255) / 256, 256, 0, stream>>>(m, dist, centers, w_rbf, w_sbf, w_down, w_up, (const float4*)acc, (uint2*)d_out, E);
}
```

```cpp
#include <hip/hip_runtime.h>
#include <hip/hip_bf16.h>
#include <stdint.h>
#include <math.h>

typedef unsigned short u16;
typedef unsigned int u32;
typedef unsigned long long u64;
typedef u32 u32x4 __attribute__((ext_vector_type(4)));
typedef u32 u32x2 __attribute__((ext_vector_type(2)));
typedef float f32x4 __attribute__((ext_vector_type(4)));
typedef float f32x2 __attribute__((ext_vector_type(2)));
typedef __bf16 bf16x2 __attribute__((ext_vector_type(2)));
typedef __attribute__((address_space(1))) u32 gu32;

#define PIN(x) asm volatile("" : "+v"(x))
#define STAMP(k, i) do {} while (0)
#define STAMPC(k, i) do {} while (0)
#pragma clang diagnostic ignored "-Wint-to-pointer-cast"
#define LDS_AS __attribute__((address_space(3)))
#define LP(T, a) ((LDS_AS T*)(u32)(a))
#define SCHED() __builtin_amdgcn_sched_barrier(0)
#define RSRC(p, bytes) __builtin_amdgcn_make_buffer_rsrc((void*)(p), 0, (bytes), 0x00020000)
#define AUX_NT 2
#define AUX_SC1 16
template <int V> struct IC { static constexpr int value = V; };

#define P1_D 2
#define ISCH {3, 3, 4, 4, 5, 5, 6}
#define GSPLIT 6
#define DEFER3 3
#define DEFER2 2
#define RSIG 1.2f
#define NEARLY 0
#define REC_AUX AUX_SC1
#define EARLY_AUX AUX_SC1
#define STA 0
#define STB 4
#define O4AT 11
#define W0AT 12
#define O4POS 0
#define SSCH {1, 2, 3, 5, 7, 8, 10}
#define KPF 1
#define R1FIRST 1
#define NREP 8

constexpr int NT = 1024;
constexpr int NWG = 256;
constexpr int NS = 7;
constexpr int NH = 8;
constexpr int NBKT = NWG * NS;
constexpr int NQ = 13;
constexpr int WQ = 1568;
constexpr int MAXW = NH * WQ;
constexpr int MAXWI = 7168;
constexpr int NSUB = 2 * NWG;
constexpr int MAXN = 7 * 4096;
constexpr int UMAX = (MAXN + 2 * NBKT) / 3 + 1;
constexpr int UPC = 10760;
constexpr int LDS_BYTES = 163840;
constexpr int P1_DUMMY = NBKT * 4;
constexpr int P1_SCR = P1_DUMMY + 256;
constexpr int P1_STG = P1_SCR + 256;
constexpr int P1_UA = (LDS_BYTES - P1_STG) / 16;
constexpr int ND = (P1_UA + NT - 1) / NT;
static_assert(UMAX < UPC && 3 * UMAX < 65535 && P1_UA % 32 == 0 && ND == 10, "geometry");
constexpr int P2_ACC = LDS_BYTES - MAXW * 8;
constexpr int LCAP = 3 * NT;
constexpr int P2_HEAD = LDS_BYTES - NS * LCAP * 4;
constexpr int P2_SCR = MAXWI * 8;
constexpr u32 AB16 = 0x4000u;
constexpr u32 ABIAS = AB16 * 0x10001u;
static_assert(P2_SCR == NT * 56 && P2_SCR + 512 <= P2_ACC && P2_ACC <= P2_HEAD && P2_ACC < 65536 && P2_ACC + 0x1fff8 >= LDS_BYTES, "layout");

constexpr size_t WS_REC = 0;
constexpr size_t WS_OFFS = WS_REC + (size_t)NSUB * UPC * 16;
constexpr size_t WS_OFFX = WS_OFFS + (size_t)NSUB * 256 * 16;
constexpr size_t WS_STAT = WS_OFFX + (size_t)NSUB * 256 * 16;
constexpr size_t WS_EPOCH = WS_STAT + 4096;
constexpr size_t WS_PHASE = WS_EPOCH + 4096;
constexpr size_t WS_FLAG = WS_PHASE + 32 * 1024;
constexpr size_t WS_PART = WS_FLAG + 256 * 128;
constexpr size_t WS_PART16 = WS_PART + (size_t)32 * 64 * WQ * 16;
constexpr size_t WS_END = WS_PART16 + (size_t)32 * 64 * WQ * 8;

__device__ __forceinline__ float bf2f(u16 v) { return __uint_as_float(((u32)v) << 16); }
__device__ __forceinline__ u32 pk_bf16(float a, float b) {
    f32x2 v = {a, b};
    bf16x2 r = __builtin_convertvector(v, bf16x2);
    return __builtin_bit_cast(u32, r);
}
__device__ __forceinline__ f32x2 rbf16x2(f32x2 v) {
    const u32 t = pk_bf16(v.x, v.y);
    return f32x2{__uint_as_float(t << 16), __uint_as_float(t & 0xffff0000u)};
}
template <int CTRL, int RM>
__device__ __forceinline__ u32 dpp0(u32 v) { return __builtin_amdgcn_update_dpp(0u, v, CTRL, RM, 0xf, false); }
__device__ __forceinline__ u32 wave_incl_sum(u32 v) {
    v += dpp0<0x111, 0xf>(v);
    v += dpp0<0x112, 0xf>(v);
    v += dpp0<0x114, 0xf>(v);
    v += dpp0<0x118, 0xf>(v);
    v += dpp0<0x142, 0xa>(v);
    v += dpp0<0x143, 0xc>(v);
    return v;
}
__device__ __forceinline__ float wave_incl_sumf(float v) {
    v += __uint_as_float(dpp0<0x111, 0xf>(__float_as_uint(v)));
    v += __uint_as_float(dpp0<0x112, 0xf>(__float_as_uint(v)));
    v += __uint_as_float(dpp0<0x114, 0xf>(__float_as_uint(v)));
    v += __uint_as_float(dpp0<0x118, 0xf>(__float_as_uint(v)));
    v += __uint_as_float(dpp0<0x142, 0xa>(__float_as_uint(v)));
    v += __uint_as_float(dpp0<0x143, 0xc>(__float_as_uint(v)));
    return v;
}
__device__ __forceinline__ u32 wave_incl_max(u32 v) {
    v = max(v, dpp0<0x111, 0xf>(v));
    v = max(v, dpp0<0x112, 0xf>(v));
    v = max(v, dpp0<0x114, 0xf>(v));
    v = max(v, dpp0<0x118, 0xf>(v));
    v = max(v, dpp0<0x142, 0xa>(v));
    v = max(v, dpp0<0x143, 0xc>(v));
    return v;
}
__device__ __forceinline__ u32 mulhi24(u32 v, u32 s) { u32 r; asm("v_mul_hi_u32_u24 %0, %1, %2" : "=v"(r) : "s"(s), "v"(v)); return r; }
__device__ __forceinline__ u32 mad24(u32 a, int s, u32 c) { u32 r; asm("v_mad_i32_i24 %0, %1, %2, %3" : "=v"(r) : "v"(a), "s"(s), "v"(c)); return r; }
__device__ __forceinline__ u32 bsel(u32 m, u32 a, u32 b) { return __builtin_amdgcn_bitop3_b32(m, a, b, 0xca); }
__device__ __forceinline__ u32 andor(u32 a, u32 m, u32 b) { return __builtin_amdgcn_bitop3_b32(a, m, b, 0xea); }
__device__ __forceinline__ u32 or3(u32 a, u32 b, u32 c) { return __builtin_amdgcn_bitop3_b32(a, b, c, 0xfe); }
__device__ __forceinline__ u32 shl3_16(u32 v) { u32 r; asm("v_lshlrev_b16 %0, 3, %1" : "=v"(r) : "v"(v)); return r; }
__device__ __forceinline__ u32 div3(u32 v) { return (u32)__umul24(v, 43691u) >> 17; }
__device__ __forceinline__ u32 lds_add_rtn(u32 addr, u32 v) { return __hip_atomic_fetch_add(LP(u32, addr), v, __ATOMIC_RELAXED, __HIP_MEMORY_SCOPE_WORKGROUP); }

__device__ __forceinline__ float cos_bf(float x) {
    const float C1 = 0.15915494309189535f;
    const float C2 = (float)(0.15915494309189533576888 - (double)0.15915494309189535f);
    float hi = x * C1;
    float lo = __builtin_fmaf(x, C1, -hi);
    lo = __builtin_fmaf(x, C2, lo);
    float r = __builtin_amdgcn_fractf(hi) + lo;
    return __builtin_amdgcn_cosf(r);
}

template <bool FULL>
__global__ void __launch_bounds__(NT)
fused_kernel(const u32x4* __restrict__ idx_in, const u32x4* __restrict__ idx_out, const u32x2* __restrict__ angle,
             const u16* __restrict__ ang_freq, const u32x4* __restrict__ m16, char* ws,
             const u16* __restrict__ dist, const u32* __restrict__ centers, const u32* __restrict__ w_rbf,
             const u32* __restrict__ w_sbf, const u32* __restrict__ w_down, const u32* __restrict__ w_up,
             u32x2* __restrict__ out, int T4, int RF, int RQ, int E, u32 W, u32 magW, u32 WI, u32 magI, float rfac)
{
    const int tid = threadIdx.x;
    const int lane = tid & 63;
    const int w = __builtin_amdgcn_readfirstlane(tid >> 6);
    const int c = blockIdx.x;
    const int m_n16 = E >> 1;
    STAMP(0, 0);
    u64 kpc; u32 klen;
    asm volatile("s_getpc_b64 %0\n.Lks%c2:\n\ts_mov_b32 %1, .Lke%c2-.Lks%c2" : "=s"(kpc), "=s"(klen) : "i"((int)FULL));
    const __amdgpu_buffer_rsrc_t rK = RSRC(kpc, klen);
    auto kfetch = [&]() {
        if (KPF && w == 13) {
            u32 t = __builtin_amdgcn_raw_buffer_load_b32(rK, (lane < 32 ? (u32)(lane * 32 + (c >> 3)) * 128u : 0xfffffff0u), 0, 0);
            asm volatile("" :: "v"(t));
        }
    };
    kfetch();
    const __amdgpu_buffer_rsrc_t rI = RSRC(idx_in, T4 * 16), rO = RSRC(idx_out, T4 * 16), rA = RSRC(angle, T4 * 8);
    const int rem0 = RF * NWG * NT + c * RQ;
    const bool valid0 = tid < min(RQ, T4 - rem0);
    auto valid = [&](int i) -> bool { return i == 0 ? valid0 : (FULL || i - 1 < RF); };
    const int mper = (m_n16 + NWG - 1) / NWG;
    const int m0i = min(c * mper, m_n16);
    const __amdgpu_buffer_rsrc_t rMs = RSRC(m16 + m0i, min(mper, m_n16 - m0i) * 16);
    const __amdgpu_buffer_rsrc_t rT = RSRC(ws + WS_OFFS, (int)(WS_FLAG - WS_OFFS));

    u32x4 rin[NQ], rout[NQ];
    u32x2 rang[NQ];
    u32 P[NQ][4], B8[NQ], BK[NQ][2];

    const u32 t16 = tid * 16, t8 = tid * 8;
    const u32 epoch = __builtin_amdgcn_raw_buffer_load_b32(rT, (int)(WS_EPOCH - WS_OFFS), 0, AUX_SC1);
    u32x4 mv = __builtin_amdgcn_raw_buffer_load_b128(rMs, t16, 0, 0);
    auto issue = [&](int i) {
        if (i == 0 || !FULL) {
            u32 q = valid(i) ? (i == 0 ? rem0 : ((i - 1) * NWG + c) * NT) + tid : 0x0fffffffu;
            rin[i] = __builtin_amdgcn_raw_buffer_load_b128(rI, q * 16, 0, AUX_NT);
            rout[i] = __builtin_amdgcn_raw_buffer_load_b128(rO, q * 16, 0, AUX_NT);
            rang[i] = __builtin_amdgcn_raw_buffer_load_b64(rA, q * 8, 0, AUX_NT);
        } else {
            const u32 qb = ((i - 1) * NWG + c) * NT;
            rin[i] = __builtin_amdgcn_raw_buffer_load_b128(rI, t16, qb * 16, AUX_NT);
            rout[i] = __builtin_amdgcn_raw_buffer_load_b128(rO, t16, qb * 16, AUX_NT);
            rang[i] = __builtin_amdgcn_raw_buffer_load_b64(rA, t8, qb * 8, AUX_NT);
        }
    };
#pragma unroll
    for (int i = 0; i < P1_D; i++) issue(i);
    const float freq = bf2f(ang_freq[0]);
    SCHED();
    const u32x4 z4 = {0, 0, 0, 0};
#pragma unroll
    for (int k = 0; k < LDS_BYTES / 16 / NT; k++) *LP(u32x4, t16 + k * NT * 16) = z4;
    __syncthreads();
    STAMP(0, 1);
    const u32 tag = __builtin_amdgcn_readfirstlane(epoch) + 0x9e3779b9u;
    auto publish_phase = [&](u32 ph) {
        if (lane < NREP) __builtin_amdgcn_raw_buffer_store_b32(tag + ph, rT, (int)(WS_PHASE - WS_OFFS) + lane * 1024 + c * 4, 0, AUX_SC1);
    };
    auto wait_phase = [&](u32 ph) {
#pragma unroll 1
        for (u32 spins = 0; spins < (1u << 20); spins++) {
            asm volatile("" ::: "memory");
            const u32x4 v = __builtin_amdgcn_raw_buffer_load_b128(rT, (int)(WS_PHASE - WS_OFFS) + (c % NREP) * 1024 + lane * 16, 0, AUX_SC1);
            const u32 t = tag + ph;
            if (__all(max(max(v.x - t, v.y - t), max(v.z - t, v.w - t)) <= 1u - ph)) break;
            __builtin_amdgcn_s_sleep(1);
        }
    };
    {
        u32 lo[4], hi[4], mx = 0;
#pragma unroll
        for (int e = 0; e < 4; e++) { lo[e] = (mv[e] << 16) & 0x7fffffffu; hi[e] = mv[e] & 0x7fff0000u; mx = max(mx, max(lo[e], hi[e])); }
        mx = wave_incl_max(mx);
        f32x4 sa = {__uint_as_float(lo[0]) + __uint_as_float(lo[2]), __uint_as_float(hi[0]) + __uint_as_float(hi[2]),
                    __uint_as_float(lo[1]) + __uint_as_float(lo[3]), __uint_as_float(hi[1]) + __uint_as_float(hi[3])};
#pragma unroll
        for (int e = 0; e < 4; e++) sa[e] = wave_incl_sumf(sa[e]);
        if (lane == 63) {
            __hip_atomic_fetch_max(LP(u32, P1_SCR + 64), mx, __ATOMIC_RELAXED, __HIP_MEMORY_SCOPE_WORKGROUP);
            *LP(float, P1_SCR + 128 + w * 4) = fmaxf(fmaxf(sa[0], sa[1]), fmaxf(sa[2], sa[3]));
        }
    }
    u32 vm8 = 0x80008000u, vmt = 0xf8000000u, v12 = 12;
    PIN(vm8); PIN(vmt); PIN(v12);

    auto encode = [&](int i) {
        float a0 = __uint_as_float(rang[i].x << 16) * freq, a1 = __uint_as_float(rang[i].x & 0xffff0000u) * freq;
        float a2 = __uint_as_float(rang[i].y << 16) * freq, a3 = __uint_as_float(rang[i].y & 0xffff0000u) * freq;
        u32 x01 = pk_bf16(a0, a1), x23 = pk_bf16(a2, a3);
        float cs[4];
        cs[0] = cos_bf(__uint_as_float(x01 << 16)); cs[1] = cos_bf(__uint_as_float(x01 & 0xffff0000u));
        cs[2] = cos_bf(__uint_as_float(x23 << 16)); cs[3] = cos_bf(__uint_as_float(x23 & 0xffff0000u));
        u32 c01 = pk_bf16(cs[0], cs[1]), c23 = pk_bf16(cs[2], cs[3]);
        B8[i] = __builtin_amdgcn_perm(c23, c01, 0x06040200u);
        const u32 t01 = bsel(vm8, c01, c01 << 3), t23 = bsel(vm8, c23, c23 << 3);
        u32 F[4] = {t01 << 16, t01, t23 << 16, t23};
        u32 bk[4];
#pragma unroll
        for (int k = 0; k < 4; k++) {
            u32 vi = rin[i][k], vo = rout[i][k];
            u32 q = mulhi24(vi, magI);
            u32 p = mulhi24(vo, magW);
            u32 li = mad24(q, -(int)WI, vi);
            u32 lo = mad24(p, -(int)W, vo);
            P[i][k] = bsel(vmt, F[k], ((lo << 13) + li) ^ 0x07ffe000u);
            bk[k] = mad24(((q & 7u) << 5) + p, NS * 4, (q >> 1) & ~3u);
            if (i == 0 || !FULL) bk[k] = valid(i) ? bk[k] : (u32)(P1_DUMMY + lane * 4);
            lds_add_rtn(bk[k], i < 7 ? 1u : 0x10000u);
        }
        BK[i][0] = bk[0] | (bk[1] << 16);
        BK[i][1] = bk[2] | (bk[3] << 16);
        PIN(BK[i][0]); PIN(BK[i][1]); PIN(B8[i]); PIN(P[i][0]); PIN(P[i][1]); PIN(P[i][2]); PIN(P[i][3]);
    };
    const __amdgpu_buffer_rsrc_t rR = RSRC(ws + WS_REC, NSUB * UPC * 16);
    auto scan = [&](int sub) -> u32 {
        u32 un[8], incl = 0;
        un[0] = 0;
        if (w < 4) {
#pragma unroll
            for (int k = 0; k < 7; k++) un[k + 1] = un[k] + div3((*LP(u32, tid * 28 + k * 4) >> (16 * sub)) + 2);
            incl = wave_incl_sum(un[7]);
            if (lane == 63) *LP(u32, P1_SCR + w * 4) = incl;
        }
        if (w == 8) *LP(u32, P1_DUMMY + lane * 4) = 0xffffu;
        __syncthreads();
        const u32x4 wt = *LP(u32x4, P1_SCR);
        const u32 U = __builtin_amdgcn_readfirstlane(wt.x + wt.y + wt.z + wt.w);
        if (w < 4) {
            const u32 base = (w > 0 ? wt.x : 0) + (w > 1 ? wt.y : 0) + (w > 2 ? wt.z : 0) + incl - un[7];
#pragma unroll
            for (int k = 0; k < 7; k++) *LP(u32, tid * 28 + k * 4) = (base + un[k]) * 3;
            u32 nn[7], sat = 0;
#pragma unroll
            for (int k = 0; k < 7; k++) { nn[k] = min(un[k + 1] - un[k], 63u); sat = max(sat, nn[k]); }
            const u32x4 o = {base | (nn[0] << 14) | (nn[1] << 20) | (nn[2] << 26), tag, nn[3] | (nn[4] << 6) | (nn[5] << 12) | (nn[6] << 18), tag};
            __builtin_amdgcn_raw_buffer_store_b128(o, rT, (sub * NWG + c) * 4096 + t16, 0, AUX_SC1);
            if (sat == 63u) {
                u32x4 x;
#pragma unroll
                for (int k = 0; k < 4; k++) x[k] = (base + un[2 * k]) | ((base + un[2 * k + 1]) << 16);
                __builtin_amdgcn_raw_buffer_store_b128(x, rT, (int)(WS_OFFX - WS_OFFS) + (sub * NWG + c) * 4096 + t16, 0, AUX_SC1);
            }
        }
        if (U > (u32)P1_UA) {
            for (u32 u = P1_UA + tid; u < U; u += NT) __builtin_amdgcn_raw_buffer_store_b128(z4, rR, ((sub * NWG + c) * UPC + u) * 16, 0, AUX_SC1);
            asm volatile("s_waitcnt vmcnt(0)" ::: "memory");
        }
        __syncthreads();
        return U;
    };
    u32 pos[NQ][4];
    auto cursor = [&](int i) {
        PIN(BK[i][0]); PIN(BK[i][1]);
        const u32 inc = valid(i) ? 1u : 0u;
        pos[i][0] = lds_add_rtn(BK[i][0] & 0xffffu, inc); pos[i][1] = lds_add_rtn(BK[i][0] >> 16, inc);
        pos[i][2] = lds_add_rtn(BK[i][1] & 0xffffu, inc); pos[i][3] = lds_add_rtn(BK[i][1] >> 16, inc);
    };
    auto scatter = [&](int i, bool big) {
#pragma unroll
        for (int k = 0; k < 4; k++) {
            const u32 ps = i < 7 ? (pos[i][k] & 0xffffu) : pos[i][k];
            const u32 t = div3(ps), zz = t >> 1;
            *LP(u32, __builtin_amdgcn_bitop3_b32((ps + t) << 2, zz, v12, 0x78) + P1_STG) = P[i][k];
            const u32 src = (k & 1) ? (B8[i] >> 8) : B8[i];
            *LP(unsigned char, __builtin_amdgcn_bitop3_b32(mad24(t, 13, ps), zz, v12, 0xf2) + P1_STG) = (unsigned char)((k & 2) ? (src >> 16) : src);
        }
        if (big) {
            const u32 g = ((i >= 7) * NWG + c) * UPC * 16;
#pragma unroll
            for (int k = 0; k < 4; k++) {
                const u32 ps = pos[i][k] & 0xffffu, t = div3(ps);
                if (t >= (u32)P1_UA && ps != 0xffffu) {
                    __builtin_amdgcn_raw_buffer_store_b32(P[i][k], rR, g + (ps + t) * 4, 0, AUX_SC1);
                    __builtin_amdgcn_raw_buffer_store_b8((unsigned char)(B8[i] >> (8 * k)), rR, g + 13 * t + ps + 12, 0, AUX_SC1);
                }
            }
        }
    };
    u32 sw1 = (tid & 8) ? ~0u : 0u, sw2 = (tid & 16) ? ~0u : 0u;
    auto unswz = [&](u32x4 v) -> u32x4 {
        u32x4 a = {bsel(sw1, v.y, v.x), bsel(sw1, v.x, v.y), bsel(sw1, v.w, v.z), bsel(sw1, v.z, v.w)};
        return u32x4{bsel(sw2, a.z, a.x), bsel(sw2, a.w, a.y), bsel(sw2, a.x, a.z), bsel(sw2, a.y, a.w)};
    };

#pragma unroll
    for (int i = 0; i < 7; i++) {
        issue(i + P1_D);
        SCHED();
        encode(i);
        SCHED();
    }
    __syncthreads();
    STAMP(0, 2);
    if (KPF > 1) kfetch();
    const u32 UA_ = scan(0);
    if (tid == 0) {
        float t = 0.f;
#pragma unroll
        for (int k = 0; k < 4; k++) { const f32x4 v = *LP(f32x4, P1_SCR + 128 + k * 16); t += (v.x + v.y) + (v.z + v.w); }
        __builtin_amdgcn_raw_buffer_store_b128(u32x4{*LP(u32, P1_SCR + 64), tag, __float_as_uint(t), tag}, rT, (int)(WS_STAT - WS_OFFS) + c * 16, 0, AUX_SC1);
    }
    STAMP(0, 3);
    {
        const bool big = UA_ > (u32)P1_UA;
        cursor(0);
#pragma unroll
        for (int i = 0; i < 7; i++) {
            if (i + 1 < 7) cursor(i + 1);
            SCHED();
            scatter(i, big);
            SCHED();
            constexpr int isch[7] = ISCH;
#pragma unroll
            for (int j = (i == 0 ? P1_D : isch[i - 1]); j < isch[i]; j++) { issue(7 + j); SCHED(); }
            if (i == 1 || i == 3 || i == 5) { encode(7 + i / 2); SCHED(); }
        }
    }
    __syncthreads();
    STAMP(0, 4);
    if (KPF > 1) kfetch();
    u32x4 DR[ND];
#pragma unroll
    for (int k = 0; k < ND; k++) {
        DR[k] = *LP(u32x4, P1_STG + t16 + k * NT * 16);
        *LP(u32x4, P1_STG + t16 + k * NT * 16) = z4;
    }
    SCHED();
    const __amdgpu_buffer_rsrc_t rS0 = RSRC(ws + WS_REC + (size_t)(u32)(c * UPC) * 16u, min(UA_, (u32)P1_UA) * 16);
    auto store0 = [&](int k) { __builtin_amdgcn_raw_buffer_store_b128(unswz(DR[k]), rS0, t16 + k * NT * 16, 0, AUX_SC1); SCHED(); };
    const int h = c & 7, p = c >> 3;
    const int task = h * 32 + p;
    const int s0 = (task & 1) ? NS - 1 : 0, sd = (task & 1) ? -1 : 1;
    const int cg = w & 7, sg = w >> 3;
    const int cc = cg * 64 + lane;
    const __amdgpu_buffer_rsrc_t rM = RSRC(m16, m_n16 * 16);
    u32x4 wr[3]; u32x2 wr3;
    auto wload = [&](int s, int k) {
        const u32 base = (u32)((s0 + sd * s) * NH + h) * WI * 8;
        if (k < 3) wr[k] = __builtin_amdgcn_raw_buffer_load_b128(rM, base + k * 16384 + t16, 0, 0);
        else wr3 = __builtin_amdgcn_raw_buffer_load_b64(rM, base + 49152 + t8, 0, 0);
    };
    auto wstore = [&](int k) {
        if (k < 3) *LP(u32x4, k * 16384 + t16) = wr[k];
        else *LP(u32x2, 49152 + t8) = wr3;
    };
    u32x4 o4, mxv = {0, tag, 0, tag};
    auto load_tables = [&]() {
        o4 = __builtin_amdgcn_raw_buffer_load_b128(rT, cc * 4096 + task * 16, 0, AUX_SC1);
        if (w < 4) mxv = __builtin_amdgcn_raw_buffer_load_b128(rT, (int)(WS_STAT - WS_OFFS) + t16, 0, AUX_SC1);
        SCHED();
    };
    encode(10); SCHED();
    encode(11); SCHED();
#pragma unroll
    for (int k = 0; k < STA; k++) store0(k);
    encode(12); SCHED();
#pragma unroll
    for (int k = STA; k < STB; k++) store0(k);
    __syncthreads();
    STAMP(0, 5);
    if (KPF > 1) kfetch();
    const u32 UB_ = scan(1);
    {
        const bool big = UB_ > (u32)P1_UA;
        cursor(7);
#pragma unroll
        for (int i = 7; i < NQ; i++) {
            if (i + 1 < NQ) cursor(i + 1);
            SCHED();
#pragma unroll
            for (int k = STB + ((i - 7) * (ND - STB)) / 6; k < STB + ((i - 6) * (ND - STB)) / 6; k++) store0(k);
            scatter(i, big);
            SCHED();
        }
    }
    __syncthreads();
    STAMP(0, 6);

#pragma unroll
    for (int k = 0; k < ND; k++) DR[k] = *LP(u32x4, P1_STG + t16 + k * NT * 16);
    const __amdgpu_buffer_rsrc_t rS1 = RSRC(ws + WS_REC + (size_t)(u32)((NWG + c) * UPC) * 16u, min(UB_, (u32)P1_UA) * 16);
    auto store1 = [&](int k) { __builtin_amdgcn_raw_buffer_store_b128(unswz(DR[k]), rS1, t16 + k * NT * 16, 0, AUX_SC1); SCHED(); };
    auto arrive = [&](int k) { SCHED(); if (lane == 0) lds_add_rtn(P1_SCR + 72 + k * 4, 1u); SCHED(); };
    auto await = [&](int k) {
        SCHED();
        while (__builtin_amdgcn_readfirstlane(__hip_atomic_load(LP(u32, P1_SCR + 72 + k * 4), __ATOMIC_RELAXED, __HIP_MEMORY_SCOPE_WORKGROUP)) < 16u)
            __builtin_amdgcn_s_sleep(1);
        asm volatile("" ::: "memory");
        SCHED();
    };
    constexpr int ssch[7] = SSCH;
    auto stores = [&](int j) {
#pragma unroll
        for (int k = (j == 0 ? 0 : ssch[j - 1]); k < ssch[j]; k++) store1(k);
    };
    load_tables();
    stores(0);
    __syncthreads();
    stores(1);
#pragma unroll
    for (int k = 0; k < 5; k++) *LP(u32x4, P2_HEAD + t16 + k * NT * 16) = z4;
    if (w < 4) *LP(u32x4, P2_HEAD + t16 + 5 * NT * 16) = z4;
    if (tid == 0) *LP(u32, P2_SCR + 400) = 0;
    SCHED();
    stores(2);
#pragma unroll 1
    for (u32 spins = 0; spins < (1u << 20) && !__all(o4.y == tag && o4.w == tag && mxv.y == tag && mxv.w == tag); spins++) {
        asm volatile("" ::: "memory");
        load_tables();
    }
    STAMP(0, 7);
    u32 n[4], inc[4], st[4];
    {
        const u32 n0 = (o4.x >> 14) & 63u, n1 = (o4.x >> 20) & 63u, n2 = o4.x >> 26, n3 = o4.z & 63u;
        n[0] = sg ? (o4.z >> 6) & 63u : n0; n[1] = sg ? (o4.z >> 12) & 63u : n1; n[2] = sg ? (o4.z >> 18) & 63u : n2; n[3] = sg ? 0 : n3;
        st[0] = (o4.x & 0x3fffu) + (sg ? n0 + n1 + n2 + n3 : 0);
        st[1] = st[0] + n[0]; st[2] = st[1] + n[1]; st[3] = st[2] + n[2];
    }
#pragma unroll
    for (int k = 0; k < 4; k++) inc[k] = wave_incl_sum(n[k] == 63u ? (u32)LCAP + 1 : n[k]);
    if (lane == 63) {
#pragma unroll
        for (int k = 0; k < 4; k++) *LP(u32, P2_SCR + ((sg * 4 + k) * 8 + cg) * 4) = inc[k];
    }
    if (w < 4) {
        const u32 mx = wave_incl_max(mxv.x);
        const float sm = wave_incl_sumf(__uint_as_float(mxv.z));
        if (lane == 63) { *LP(u32, P2_SCR + 384 + w * 4) = mx; *LP(float, P2_SCR + 416 + w * 4) = sm; }
    }
    asm volatile("" ::: "memory");
    arrive(0);
    stores(3);
    await(0);
    STAMP(1, 21);
    u32 tot_l;
    {
        const u32 ta = *LP(u32, P2_SCR + lane * 4);
        u32 ia = ta;
        { u32 t = dpp0<0x111, 0xf>(ia); ia += (lane & 7) >= 1 ? t : 0; }
        { u32 t = dpp0<0x112, 0xf>(ia); ia += (lane & 7) >= 2 ? t : 0; }
        { u32 t = dpp0<0x114, 0xf>(ia); ia += (lane & 7) >= 4 ? t : 0; }
        tot_l = ia;
        ia -= ta;
        bool more = false;
        u32 nx[4], en[4], dl[4];
#pragma unroll
        for (int k = 0; k < 4; k++) {
            const int s = sg * 4 + k;
            const u32 carry = __builtin_amdgcn_readlane(ia, s * 8 + cg);
            u32 ex = carry + inc[k] - n[k];
            en[k] = min(carry + inc[k], (u32)LCAP);
            dl[k] = ((u32)cc * UPC + st[k] - ex) * 16;
            const u32 hb = P2_HEAD + s * LCAP * 4;
            *LP(u32, ex < en[k] ? hb + ex * 4 : LDS_BYTES) = dl[k];
            const u32 b1 = (ex | 7u) + 1;
            *LP(u32, b1 < en[k] ? hb + b1 * 4 : LDS_BYTES) = dl[k];
            nx[k] = b1 + 8;
            more |= nx[k] < en[k];
        }
        if (more) {
#pragma unroll
            for (int k = 0; k < 4; k++)
                for (u32 ps = nx[k]; ps < en[k]; ps += 8) *LP(u32, P2_HEAD + (sg * 4 + k) * LCAP * 4 + ps * 4) = dl[k];
        }
        if (w < NS) {
            const u32 tt = __builtin_amdgcn_readlane(tot_l, w * 8 + 7);
            for (u32 ps = (tt & ~7u) + lane * 8; ps < (u32)LCAP; ps += 512) *LP(u32, P2_HEAD + (w * LCAP + max(ps, tt)) * 4) = 0xf0000000u;
        }
    }
    u32x4 mq = *LP(u32x4, P2_SCR + 384);
    const u32 maxbits = __builtin_amdgcn_readfirstlane(max(max(mq[0], mq[1]), max(mq[2], mq[3])));
    const f32x4 sq = *LP(f32x4, P2_SCR + 416);
    const float range = fmaxf(rfac * ((sq.x + sq.y) + (sq.z + sq.w)), 2.0f * __uint_as_float(maxbits));
    bool slow = __any(tot_l > (u32)LCAP) || maxbits < 0x10000000u || maxbits > 0x70000000u;
    const bool early = NEARLY > 0 && !__any((lane & 7) == 3 && lane < NS * 8 && tot_l < (u32)NT);
    asm volatile("s_waitcnt vmcnt(%0)" :: "i"(ssch[3]) : "memory");
    arrive(1);
    stores(4);
    await(1);
    if (w == 14) publish_phase(0);
    STAMP(1, 6);
    u32 hv[NS][3];
#pragma unroll
    for (int s = 0; s < NS; s++)
#pragma unroll
        for (int t = 0; t < 3; t++) hv[s][t] = *LP(u32, P2_HEAD + ((s0 + sd * s) * LCAP + t * NT + tid) * 4);
    asm volatile("" ::: "memory");
    arrive(2);
    stores(5);
#pragma unroll
    for (int s = 0; s < NS; s++)
#pragma unroll
        for (int t = 0; t < 3; t++) {
            u32 v = hv[s][t];
            v = max(v, dpp0<0x111, 0xf>(v));
            v = max(v, dpp0<0x112, 0xf>(v));
            v = max(v, dpp0<0x114, 0xf>(v));
            hv[s][t] = v + t16 + t * NT * 16;
        }
#pragma unroll
    for (int s = 0; s < NS; s++)
#pragma unroll
        for (int t = 0; t < 3; t++) PIN(hv[s][t]);
    await(2);
    {
        const u32x4 b4 = {ABIAS, ABIAS, ABIAS, ABIAS};
#pragma unroll
        for (int k = 0; k < 6; k++) *LP(u32x4, P2_ACC + t16 + k * NT * 16) = b4;
        if (w < 2) *LP(u32x4, P2_ACC + t16 + 6 * NT * 16) = b4;
    }
    SCHED();
    stores(6);
    STAMP(1, 7);
    asm volatile("" :: "s"(centers[0]), "s"(w_sbf[0]), "s"(w_rbf[0]), "s"(w_down[0]), "s"(w_up[0]));
    if (w == 15 && early) wait_phase(0);
    asm volatile("s_waitcnt vmcnt(0) lgkmcnt(0)\n\ts_barrier" ::: "memory");
    if (w == 14) publish_phase(1);
    STAMP(1, 1);

    const float quantum = range * (1.0f / AB16);
    u32 ovf = 0;
    auto handoff = [&]() {
        if (w == 15) wait_phase(1);
        asm volatile("s_waitcnt lgkmcnt(0)\n\ts_barrier" ::: "memory");
        if (c == 0 && tid == 0) __builtin_amdgcn_raw_buffer_store_b32(tag, rT, (int)(WS_EPOCH - WS_OFFS), 0, AUX_SC1);
    };
    if (!slow) {
        float S = (float)AB16 / range;
        float MAG = 8421376.0f;
        u32 vmf = 0x8f000000u, v30 = 0x30000000u, vma = 0x1fff8u;
        PIN(MAG); PIN(vmf); PIN(v30); PIN(S); PIN(vma);
        auto body = [&](auto NC) {
            constexpr int NU = decltype(NC)::value;
            constexpr int NR = 3 * NU;
            constexpr int ND_ = NU == 3 ? DEFER3 : DEFER2;
            u32x4 RP[NS][NU];
            u32x2 G[NS][NR];
            u64 OLD[NS][NR];
#pragma unroll
            for (int k = 0; k < 4; k++) wload(0, k);
            SCHED();
#pragma unroll
            for (int k = 0; k < 4; k++) { wstore(k); SCHED(); wload(1, k); SCHED(); }
            STAMP(1, 2);
            if (!early) handoff();
#pragma unroll
            for (int s = 0; s < NEARLY; s++) RP[s][0] = __builtin_amdgcn_raw_buffer_load_b128(rR, hv[s][0], 0, EARLY_AUX);
            SCHED();
            if (early) handoff();
            STAMP(1, 5);
            auto rload = [&](int s) {
                if (s < NS) {
#pragma unroll
                    for (int t = (s < NEARLY ? 1 : 0); t < NU; t++) RP[s][t] = __builtin_amdgcn_raw_buffer_load_b128(rR, hv[s][t], 0, REC_AUX);
                }
            };
            rload(0);
            auto accum = [&](int s, int r) {
                const int t = r / 3, k = r % 3;
                u32 Pk = RP[s][t][k];
                u32 lowb = __builtin_amdgcn_perm(v30, RP[s][t][3], 0x07000c0cu | ((u32)k << 16));
                float cs = __uint_as_float(andor((u32)((int)Pk >> 3), vmf, lowb)) * S;
                u32 g0 = G[s][r].x, g1 = G[s][r].y;
                u32 x0 = __float_as_uint(__builtin_fmaf(__uint_as_float(g0 << 16), cs, MAG));
                u32 x1 = __float_as_uint(__builtin_fmaf(__uint_as_float(g0 & 0xffff0000u), cs, MAG));
                u32 x2 = __float_as_uint(__builtin_fmaf(__uint_as_float(g1 << 16), cs, MAG));
                u32 x3 = __float_as_uint(__builtin_fmaf(__uint_as_float(g1 & 0xffff0000u), cs, MAG));
                u32x2 pa = {__builtin_amdgcn_perm(x1, x0, 0x05040100u), __builtin_amdgcn_perm(x3, x2, 0x05040100u)};
                u64 a = __builtin_bit_cast(u64, pa) - 0x8000800080008000ull;
                u32 ad = __builtin_amdgcn_bitop3_b32(Pk >> 10, vma, vma, 0x08);
                OLD[s][r] = __hip_atomic_fetch_add(LP(u64, ad + P2_ACC), a, __ATOMIC_RELAXED, __HIP_MEMORY_SCOPE_WORKGROUP);
            };
            auto check = [&](int q) {
                if (q >= 0) {
                    u32x2 y = __builtin_bit_cast(u32x2, OLD[q / NR][q % NR]);
                    ovf = or3(y.x, y.y, ovf);
                    PIN(ovf);
                }
            };
            constexpr int LAG = 2;
#pragma unroll
            for (int s = 0; s < NS; s++) {
                SCHED();
                if (s > 0) asm volatile("s_waitcnt lgkmcnt(1)\n\ts_barrier" ::: "memory");
                if (s == 0 && R1FIRST) rload(1);
#pragma unroll
                for (int r = 0; r < NR; r++) G[s][r] = *LP(u32x2, shl3_16(RP[s][r / 3][r % 3]));
                SCHED();
                if (s > 0 || !R1FIRST) rload(s + 1);
                SCHED();
                if (s > 0) {
#pragma unroll
                    for (int r = NR - ND_; r < NR; r++) { accum(s - 1, r); SCHED(); }
                }
                if (s + 1 < NS) {
                    if (s == 0) asm volatile("s_waitcnt lgkmcnt(0)\n\ts_barrier" ::: "memory");
                    else asm volatile("s_waitcnt lgkmcnt(%0)\n\ts_barrier" :: "i"(ND_) : "memory");
                }
                if (s == 0) { STAMP(1, 3); STAMPC(1, 30); }
                if (s > 0) {
#pragma unroll
                    for (int r = NR - ND_; r < NR; r++) check((s - 1) * NR + r - LAG);
                }
                constexpr int NA = NR - ND_;
#pragma unroll
                for (int r = 0; r < NA; r++) {
#pragma unroll
                    for (int k = 0; k < 4; k++) if (r == (k * (NA - 1)) / 3) {
                        if (s + 1 < NS) { wstore(k); SCHED(); }
                        if (s + 2 < NS) { wload(s + 2, k); SCHED(); }
                    }
                    check(s * NR + r - LAG);
                    accum(s, r);
                    SCHED();
                }
            }
#pragma unroll
            for (int r = NR - ND_; r < NR; r++) accum(NS - 1, r);
#pragma unroll
            for (int q = NS * NR - ND_ - LAG; q < NS * NR; q++) check(q);
        };
        if (__any(tot_l > (u32)(2 * NT + w * 64))) body(IC<3>{});
        else body(IC<2>{});
    } else {
        handoff();
    }
    const u32 e0 = (u32)p * W + (u32)h * WQ;
    const u32 ne = min(min((u32)WQ, W - min(W, (u32)h * WQ)), (u32)E - min((u32)E, e0));
    const __amdgpu_buffer_rsrc_t rD = RSRC(dist, E * 2);
    const bool act = w * 128 < WQ;
    u32x4 em = {0, 0, 0, 0}; u32 ed = 0;
    if (act) {
        em = __builtin_amdgcn_raw_buffer_load_b128(rM, (e0 + tid * 2) * 8, 0, 0);
        ed = __builtin_amdgcn_raw_buffer_load_b32(rD, (e0 + tid * 2) * 2, 0, 0);
    }
    __syncthreads();
    STAMP(1, 19); STAMPC(1, 31);

    u32 cw[4], wsb[16], wrb[16], wdn[8], wup[8];
#pragma unroll
    for (int k = 0; k < 4; k++) cw[k] = centers[k];
#pragma unroll
    for (int k = 0; k < 16; k++) { wsb[k] = w_sbf[k]; wrb[k] = w_rbf[k]; }
#pragma unroll
    for (int k = 0; k < 8; k++) { wdn[k] = w_down[k]; wup[k] = w_up[k]; }
    const __amdgpu_buffer_rsrc_t rX = RSRC(ws + WS_PART + (size_t)p * 64 * WQ * 16, 64 * WQ * 16);
    const __amdgpu_buffer_rsrc_t rY = RSRC(ws + WS_PART16 + (size_t)p * 64 * WQ * 8, 64 * WQ * 8);
    u32x4 own = {ABIAS, ABIAS, ABIAS, ABIAS};
    if (!slow) {
        if (tid < WQ / 2) {
#pragma unroll
            for (int q = 1; q < NH; q++) {
                const u32 hd = (h + q) & 7;
                const u32x4 v = *LP(u32x4, P2_ACC + (hd * WQ + tid * 2) * 8);
                __builtin_amdgcn_raw_buffer_store_b128(v, rY, ((hd * 8 + h) * WQ + tid * 2) * 8, 0, AUX_SC1);
                ovf = or3(v.x, v.y, or3(v.z, v.w, ovf));
            }
            own = *LP(u32x4, P2_ACC + (h * WQ + tid * 2) * 8);
            ovf = or3(own.x, own.y, or3(own.z, own.w, ovf));
        }
        if (ovf & 0x80008000u) *LP(u32, P2_SCR + 400) = 1;
    }
    SCHED();
    f32x2 gs[4], hs[4];
    auto gates = [&](int j0, int j1) {
        const f32x2 d = {__uint_as_float(ed << 16), __uint_as_float(ed & 0xffff0000u)};
#pragma unroll
        for (int j = j0; j < j1; j++) {
            const float ce = __uint_as_float((j & 1) ? (cw[j >> 1] & 0xffff0000u) : (cw[j >> 1] << 16));
            f32x2 df = rbf16x2(d - ce);
            f32x2 sq2 = rbf16x2(df * df);
            f32x2 ar = rbf16x2(sq2 * -10.0f) * 1.4426950408889634f;
            f32x2 rb = rbf16x2(f32x2{__builtin_amdgcn_exp2f(ar.x), __builtin_amdgcn_exp2f(ar.y)});
#pragma unroll
            for (int k = 0; k < 4; k++) {
                const u32 ws_ = wsb[j * 2 + (k >> 1)], wr_ = wrb[j * 2 + (k >> 1)];
                gs[k] += rb * __uint_as_float((k & 1) ? (ws_ & 0xffff0000u) : (ws_ << 16));
                hs[k] += rb * __uint_as_float((k & 1) ? (wr_ & 0xffff0000u) : (wr_ << 16));
            }
        }
    };
#pragma unroll
    for (int k = 0; k < 4; k++) { gs[k] = f32x2{0.f, 0.f}; hs[k] = f32x2{0.f, 0.f}; }
    if (act) gates(0, GSPLIT);
#pragma unroll
    for (int k = 0; k < 4; k++) { PIN(gs[k]); PIN(hs[k]); }
    asm volatile("s_waitcnt vmcnt(0)" ::: "memory");
    __syncthreads();
    f32x4 sm0 = {0, 0, 0, 0}, sm1 = {0, 0, 0, 0};
    slow = slow || __builtin_amdgcn_readfirstlane(*LP(u32, P2_SCR + 400)) != 0;
    if (slow) {
        const u16* m2 = (const u16*)m16;
        for (int cmp = 0; cmp < 4; cmp++) {
            __syncthreads();
            for (u32 k = tid; k < MAXW / 2; k += NT) *LP(u32x4, P2_ACC + k * 16) = z4;
            __syncthreads();
            for (int r = w; r < NSUB * NS; r += 16) {
                const int ck = r / NS, s = r - ck * NS;
                const u32x4 oe = __builtin_amdgcn_raw_buffer_load_b128(rT, ck * 4096 + task * 16, 0, AUX_SC1);
                const u64 cn = (u64)(oe.x >> 14) | ((u64)oe.z << 18);
                u32 b0 = oe.x & 0x3fffu, b1 = 0;
                bool sat = false;
                for (int k = 0; k < NS; k++) { const u32 nk = (u32)(cn >> (6 * k)) & 63u; sat |= nk == 63u; if (k < s) b0 += nk; if (k == s) b1 = nk; }
                b1 += b0;
                if (sat) {
                    const u32 xa = (u32)(WS_OFFX - WS_OFFS) + ck * 4096 + task * 16 + s * 2;
                    b0 = (u16)__builtin_amdgcn_raw_buffer_load_b16(rT, xa, 0, AUX_SC1);
                    b1 = (u16)__builtin_amdgcn_raw_buffer_load_b16(rT, xa + 2, 0, AUX_SC1);
                }
                b0 += (u32)ck * UPC; b1 += (u32)ck * UPC;
                const u32 inb = (u32)(s * NH + h) * WI;
                for (u32 j = b0 + lane; j < b1; j += 64) {
                    const u32x4 un = __builtin_amdgcn_raw_buffer_load_b128(rR, j * 16, 0, AUX_SC1);
#pragma unroll
                    for (int k = 0; k < 3; k++) {
                        const u32 Pk = un[k];
                        if (Pk == 0) continue;
                        u32 F = (((u32)((int)Pk >> 3)) & 0x8f000000u) | 0x30000000u | (((un[3] >> (8 * k)) & 0xffu) << 16);
                        if ((F & 0x7fffffffu) == 0x30000000u) F &= 0x80000000u;
                        if ((F & 0x7fffffffu) > 0x3f800000u) F = 0x7fc00000u;
                        const float mm = bf2f(m2[(size_t)(inb + (Pk & 0x1fffu)) * 4 + cmp]);
                        __hip_atomic_fetch_add(LP(double, P2_ACC + (~(Pk >> 10) & 0x1fff8u)), (double)__uint_as_float(F) * (double)mm,
                                               __ATOMIC_RELAXED, __HIP_MEMORY_SCOPE_WORKGROUP);
                    }
                }
            }
            __syncthreads();
            for (u32 e = tid; e < MAXW; e += NT) {
                const u32 hd = e / WQ, el = e - hd * WQ;
                __builtin_amdgcn_raw_buffer_store_b32(__float_as_uint((float)*LP(double, P2_ACC + e * 8)), rX, ((hd * 8 + h) * WQ + el) * 16 + cmp * 4, 0, AUX_SC1);
            }
            if (tid < WQ / 2) {
                const float a = (float)*LP(double, P2_ACC + (h * WQ + tid * 2) * 8), b = (float)*LP(double, P2_ACC + (h * WQ + tid * 2) * 8 + 8);
                sm0 = f32x4{cmp == 0 ? a : sm0.x, cmp == 1 ? a : sm0.y, cmp == 2 ? a : sm0.z, cmp == 3 ? a : sm0.w};
                sm1 = f32x4{cmp == 0 ? b : sm1.x, cmp == 1 ? b : sm1.y, cmp == 2 ? b : sm1.z, cmp == 3 ? b : sm1.w};
            }
        }
        own = u32x4{ABIAS, ABIAS, ABIAS, ABIAS};
        asm volatile("s_waitcnt vmcnt(0)" ::: "memory");
        __syncthreads();
    }
    gu32* flags = (gu32*)(ws + WS_FLAG);
    if (tid == NT - 64) __hip_atomic_store(flags + task * 32, tag + (slow ? 1u : 0u), __ATOMIC_RELAXED, __HIP_MEMORY_SCOPE_AGENT);
    STAMP(1, 20);
    if (act) {
        gates(GSPLIT, 8);
#pragma unroll
        for (int k = 0; k < 4; k++) hs[k] = rbf16x2(hs[k]);
    }
#pragma unroll
    for (int k = 0; k < 4; k++) { PIN(gs[k]); PIN(hs[k]); }
#pragma unroll
    for (int k = 0; k < 8; k++) asm volatile("" : "+s"(wdn[k]), "+s"(wup[k]));
    STAMP(1, 24);
    if (w == 15) {
        gu32* f = flags + (((h + 1 + min(lane, 6)) & 7) * 32 + p) * 32;
        u32 v = 0;
        for (u32 spins = 0; spins < (1u << 22); spins++) {
            v = __hip_atomic_load(f, __ATOMIC_RELAXED, __HIP_MEMORY_SCOPE_AGENT) - tag;
            if (__all(v <= 1u)) break;
            __builtin_amdgcn_s_sleep(1);
        }
        const u64 md = __ballot(v == 1u);
        if (lane == 0) *LP(u32, P2_SCR + 404) = (u32)md & 0x7fu;
    }
    __syncthreads();
    STAMP(1, 22);
    if (tid * 2 < ne) {
        const u32 modes = __builtin_amdgcn_readfirstlane(*LP(u32, P2_SCR + 404));
        u32x4 lo = own & 0xffffu, hi = own >> 16;
        if (modes == 0) {
            u32x4 x[NH - 1];
#pragma unroll
            for (int q = 1; q < NH; q++)
                x[q - 1] = __builtin_amdgcn_raw_buffer_load_b128(rY, ((h * 8 + ((h + q) & 7)) * WQ + tid * 2) * 8, 0, AUX_SC1);
#pragma unroll
            for (int q = 0; q < NH - 1; q++) { lo += x[q] & 0xffffu; hi += x[q] >> 16; }
        } else {
            for (int q = 1; q < NH; q++) {
                const u32 idx = (h * 8 + ((h + q) & 7)) * WQ + tid * 2;
                u32x4 x = {ABIAS, ABIAS, ABIAS, ABIAS};
                if ((modes >> (q - 1)) & 1) {
                    sm0 += __builtin_bit_cast(f32x4, __builtin_amdgcn_raw_buffer_load_b128(rX, idx * 16, 0, AUX_SC1));
                    sm1 += __builtin_bit_cast(f32x4, __builtin_amdgcn_raw_buffer_load_b128(rX, idx * 16 + 16, 0, AUX_SC1));
                } else x = __builtin_amdgcn_raw_buffer_load_b128(rY, idx * 8, 0, AUX_SC1);
                lo += x & 0xffffu; hi += x >> 16;
            }
        }
        lo -= NH * AB16; hi -= NH * AB16;
        if (modes != 0x7f || !slow) {
            sm0 += f32x4{(float)(int)lo.x, (float)(int)hi.x, (float)(int)lo.y, (float)(int)hi.y} * quantum;
            sm1 += f32x4{(float)(int)lo.z, (float)(int)hi.z, (float)(int)lo.w, (float)(int)hi.w} * quantum;
        }
        STAMP(1, 25);
        f32x2 agg[4], o[4];
#pragma unroll
        for (int k = 0; k < 4; k++) {
            f32x2 v = {0.f, 0.f};
#pragma unroll
            for (int j = 0; j < 4; j++) {
                const u32 wd = wdn[j * 2 + (k >> 1)];
                v += f32x2{sm0[j], sm1[j]} * __uint_as_float((k & 1) ? (wd & 0xffff0000u) : (wd << 16));
            }
            agg[k] = rbf16x2(v * gs[k]);
        }
        const f32x2 mm[4] = {f32x2{__uint_as_float(em.x << 16), __uint_as_float(em.z << 16)},
                             f32x2{__uint_as_float(em.x & 0xffff0000u), __uint_as_float(em.z & 0xffff0000u)},
                             f32x2{__uint_as_float(em.y << 16), __uint_as_float(em.w << 16)},
                             f32x2{__uint_as_float(em.y & 0xffff0000u), __uint_as_float(em.w & 0xffff0000u)}};
#pragma unroll
        for (int k = 0; k < 4; k++) {
            f32x2 v = {0.f, 0.f};
#pragma unroll
            for (int j = 0; j < 4; j++) {
                const u32 wu = wup[j * 2 + (k >> 1)];
                v += agg[j] * __uint_as_float((k & 1) ? (wu & 0xffff0000u) : (wu << 16));
            }
            o[k] = mm[k] + rbf16x2(rbf16x2(v) * hs[k]);
        }
        u32x4 ov = {pk_bf16(o[0].x, o[1].x), pk_bf16(o[2].x, o[3].x), pk_bf16(o[0].y, o[1].y), pk_bf16(o[2].y, o[3].y)};
        *(u32x4*)(out + e0 + tid * 2) = ov;
    }
    STAMP(1, 23);
    asm volatile(".Lke%c0:" :: "i"((int)FULL));
}

extern "C" void kernel_launch(void* const* d_in, const int* in_sizes, int n_in,
                              void* d_out, int out_size, void* d_ws, size_t ws_size,
                              hipStream_t stream) {
    const int E = in_sizes[3];
    const int T = in_sizes[1];
    u32 W = (u32)((E + 31) / 32); W = (W + 1) & ~1u;
    u32 WI = (u32)((E + NH * NS - 1) / (NH * NS)); WI = (WI + 1) & ~1u;
    u32 magW = (u32)((0x100000000ull + W - 1) / W);
    u32 magI = (u32)((0x100000000ull + WI - 1) / WI);
    int T4 = T / 4;
    int RF = T4 / (NWG * NT);
    int RQ = (T4 - RF * NWG * NT + NWG - 1) / NWG;
    const float rfac = RSIG * (7.0f + 4.5f * sqrtf((float)T / ((float)E * NH))) / (float)E;
    auto kf = RF == NQ - 1 ? fused_kernel<true> : fused_kernel<false>;
    (void)hipFuncSetAttribute((const void*)kf, hipFuncAttributeMaxDynamicSharedMemorySize, LDS_BYTES);
    kf<<<NWG, NT, LDS_BYTES, stream>>>((const u32x4*)d_in[1], (const u32x4*)d_in[2], (const u32x2*)d_in[4],
        (const u16*)d_in[6], (const u32x4*)d_in[0], (char*)d_ws, (const u16*)d_in[3],
        (const u32*)d_in[5], (const u32*)d_in[7], (const u32*)d_in[8], (const u32*)d_in[9], (const u32*)d_in[10],
        (u32x2*)d_out, T4, RF, RQ, E, W, magW, WI, magI, rfac);
}
```

```cpp
#include <hip/hip_runtime.h>
#include <hip/hip_bf16.h>
#include <stdint.h>
#include <math.h>

typedef unsigned short u16;
typedef unsigned int u32;
typedef unsigned long long u64;
typedef u32 u32x4 __attribute__((ext_vector_type(4)));
typedef u32 u32x2 __attribute__((ext_vector_type(2)));
typedef float f32x4 __attribute__((ext_vector_type(4)));
typedef float f32x2 __attribute__((ext_vector_type(2)));
typedef __bf16 bf16x2 __attribute__((ext_vector_type(2)));
typedef __attribute__((address_space(1))) u32 gu32;

#pragma clang diagnostic ignored "-Wint-to-pointer-cast"
#define PIN(x) asm volatile("" : "+v"(x))
#define LP(T, a) ((__attribute__((address_space(3))) T*)(u32)(a))
#define SCHED() __builtin_amdgcn_sched_barrier(0)
#define RSRC(p, bytes) __builtin_amdgcn_make_buffer_rsrc((void*)(p), 0, (bytes), 0x00020000)
#define AUX_NT 2
#define AUX_SC1 16

constexpr int NT = 1024;
constexpr int NWG = 256;
constexpr int NS = 7;
constexpr int NH = 8;
constexpr int NBKT = NWG * NS;
constexpr int NQ = 13;
constexpr int WQ = 1568;
constexpr int MAXW = NH * WQ;
constexpr int MAXWI = 7168;
constexpr int NSUB = 2 * NWG;
constexpr int MAXN = 7 * 4096;
constexpr int UMAX = (MAXN + 2 * NBKT) / 3 + 1;
constexpr int UPC = 10760;
constexpr int LDS_BYTES = 163840;
constexpr int P1_DUMMY = NBKT * 4;
constexpr int P1_SCR = P1_DUMMY + 256;
constexpr int P1_STG = P1_SCR + 256;
constexpr int P1_UA = (LDS_BYTES - P1_STG) / 16;
constexpr int ND = (P1_UA + NT - 1) / NT;
static_assert(UMAX < UPC && 3 * UMAX < 65535 && P1_UA % 32 == 0 && ND == 10, "geometry");
constexpr int P2_ACC = LDS_BYTES - MAXW * 8;
constexpr int LCAP = 3 * NT;
constexpr int P2_HEAD = LDS_BYTES - NS * LCAP * 4;
constexpr int P2_SCR = MAXWI * 8;
constexpr u32 AB16 = 0x4000u;
constexpr u32 ABIAS = AB16 * 0x10001u;
static_assert(P2_SCR == NT * 56 && P2_SCR + 1024 <= P2_ACC && P2_ACC <= P2_HEAD && P2_ACC < 65536 && P2_ACC + 0x1fff8 >= LDS_BYTES, "layout");

constexpr size_t WS_REC = 0;
constexpr size_t WS_OFFS = WS_REC + (size_t)NSUB * UPC * 16;
constexpr size_t WS_OFFX = WS_OFFS + (size_t)NSUB * 256 * 32;
constexpr size_t WS_STAT = WS_OFFX + (size_t)NSUB * 256 * 32;
constexpr size_t WS_EPOCH = WS_STAT + 4096;
constexpr size_t WS_PHASE = WS_EPOCH + 4096;
constexpr size_t WS_FLAG = WS_PHASE + 8 * 1024;
constexpr size_t WS_PART = WS_FLAG + 32 * 16 * 128;
constexpr size_t WS_PART16 = WS_PART + (size_t)32 * 64 * WQ * 16;
constexpr size_t WS_END = WS_PART16 + (size_t)32 * 64 * WQ * 8;

__device__ __forceinline__ float bf2f(u16 v) { return __uint_as_float(((u32)v) << 16); }
__device__ __forceinline__ u32 pk_bf16(float a, float b) {
    f32x2 v = {a, b};
    bf16x2 r = __builtin_convertvector(v, bf16x2);
    return __builtin_bit_cast(u32, r);
}
__device__ __forceinline__ float rbf16(float v) { return __uint_as_float(pk_bf16(v, v) << 16); }
__device__ __forceinline__ f32x2 rbf16x2(f32x2 v) {
    const u32 t = pk_bf16(v.x, v.y);
    return f32x2{__uint_as_float(t << 16), __uint_as_float(t & 0xffff0000u)};
}
template <int CTRL, int RM>
__device__ __forceinline__ u32 dpp0(u32 v) { return __builtin_amdgcn_update_dpp(0u, v, CTRL, RM, 0xf, false); }
__device__ __forceinline__ u32 row_incl_sum(u32 v) {
    v += dpp0<0x111, 0xf>(v);
    v += dpp0<0x112, 0xf>(v);
    v += dpp0<0x114, 0xf>(v);
    v += dpp0<0x118, 0xf>(v);
    return v;
}
__device__ __forceinline__ u32 half_incl_sum(u32 v) {
    v = row_incl_sum(v);
    return v + dpp0<0x142, 0xa>(v);
}
__device__ __forceinline__ float wave_incl_sumf(float v) {
    v += __uint_as_float(dpp0<0x111, 0xf>(__float_as_uint(v)));
    v += __uint_as_float(dpp0<0x112, 0xf>(__float_as_uint(v)));
    v += __uint_as_float(dpp0<0x114, 0xf>(__float_as_uint(v)));
    v += __uint_as_float(dpp0<0x118, 0xf>(__float_as_uint(v)));
    v += __uint_as_float(dpp0<0x142, 0xa>(__float_as_uint(v)));
    v += __uint_as_float(dpp0<0x143, 0xc>(__float_as_uint(v)));
    return v;
}
__device__ __forceinline__ u32 wave_incl_max(u32 v) {
    v = max(v, dpp0<0x111, 0xf>(v));
    v = max(v, dpp0<0x112, 0xf>(v));
    v = max(v, dpp0<0x114, 0xf>(v));
    v = max(v, dpp0<0x118, 0xf>(v));
    v = max(v, dpp0<0x142, 0xa>(v));
    v = max(v, dpp0<0x143, 0xc>(v));
    return v;
}
__device__ __forceinline__ u32 mulhi24(u32 v, u32 s) { u32 r; asm("v_mul_hi_u32_u24 %0, %1, %2" : "=v"(r) : "s"(s), "v"(v)); return r; }
__device__ __forceinline__ u32 mad24(u32 a, int s, u32 c) { u32 r; asm("v_mad_i32_i24 %0, %1, %2, %3" : "=v"(r) : "v"(a), "s"(s), "v"(c)); return r; }
__device__ __forceinline__ u32 bsel(u32 m, u32 a, u32 b) { return __builtin_amdgcn_bitop3_b32(m, a, b, 0xca); }
__device__ __forceinline__ u32 andor(u32 a, u32 m, u32 b) { return __builtin_amdgcn_bitop3_b32(a, m, b, 0xea); }
__device__ __forceinline__ u32 or3(u32 a, u32 b, u32 c) { return __builtin_amdgcn_bitop3_b32(a, b, c, 0xfe); }
__device__ __forceinline__ u32 shl3_16(u32 v) { u32 r; asm("v_lshlrev_b16 %0, 3, %1" : "=v"(r) : "v"(v)); return r; }
__device__ __forceinline__ u32 shl2_16(u32 v) { u32 r; asm("v_lshlrev_b16 %0, 2, %1" : "=v"(r) : "v"(v)); return r; }
__device__ __forceinline__ u32 div3(u32 v) { return (u32)__umul24(v, 43691u) >> 17; }
__device__ __forceinline__ u32 lds_add_rtn(u32 addr, u32 v) { return __hip_atomic_fetch_add(LP(u32, addr), v, __ATOMIC_RELAXED, __HIP_MEMORY_SCOPE_WORKGROUP); }
__device__ __forceinline__ float half_of(u32 w, int k) { return __uint_as_float((k & 1) ? (w & 0xffff0000u) : (w << 16)); }

__device__ __forceinline__ float rev_large(float x) {
    const u32 b = __float_as_uint(x) & 0x7fffffffu;
    const u32 k = min((b >> 23) - 134u, 127u), j = k >> 5;
    const u32 M = ((b >> 16) & 0x7fu) | 0x80u;
    const u32 hi = j == 0 ? 0x28be60dbu : j == 1 ? 0x9391054au : j == 2 ? 0x7f09d5f4u : 0x7d4d3770u;
    const u32 lo = j == 0 ? 0x9391054au : j == 1 ? 0x7f09d5f4u : j == 2 ? 0x7d4d3770u : 0x36d8a566u;
    const u32 f = (u32)(((((u64)hi << 32) | lo) << (k & 31u)) >> 32);
    const float r = (float)(M * f) * 2.3283064365386963e-10f;
    return b >= 0x7f800000u ? __uint_as_float(0x7fc00000u) : r;
}

template <bool FULL>
__global__ void __launch_bounds__(NT)
gemnet_fused_kernel(const u32x4* __restrict__ idx_in, const u32x4* __restrict__ idx_out, const u32x2* __restrict__ angle,
                    const u16* __restrict__ ang_freq, const u32x4* __restrict__ m16, char* ws,
                    const u16* __restrict__ dist, const u32* __restrict__ centers, const u32* __restrict__ w_rbf,
                    const u32* __restrict__ w_sbf, const u32* __restrict__ w_down, const u32* __restrict__ w_up,
                    u32x2* __restrict__ out, int T4, int RF, int RQ, int E, u32 W, u32 magW, u32 WI, u32 magI, float rfac)
{
    const int tid = threadIdx.x;
    const int lane = tid & 63;
    const int w = __builtin_amdgcn_readfirstlane(tid >> 6);
    const int c = blockIdx.x;
    const int m_n16 = E >> 1;
    u32 warm = 0;
    {
        u64 kpc; u32 klen;
        asm volatile("s_getpc_b64 %0\n.Lks%c2:\n\ts_mov_b32 %1, .Lke%c2-.Lks%c2" : "=s"(kpc), "=s"(klen) : "i"((int)FULL));
        if (w == 13) warm = __builtin_amdgcn_raw_buffer_load_b32(RSRC(kpc, klen), lane < 32 ? (u32)(lane * 32 + (c >> 3)) * 128u : 0xfffffff0u, 0, 0);
        if (w == 12) {
            const u32 off = lane < 16 ? (u32)(WS_FLAG - WS_STAT) + ((c >> 3) * 16 + lane) * 128
                          : lane < 24 ? (u32)(WS_PHASE - WS_STAT) + (c & 7) * 1024 + (lane - 16) * 128 : (c >> 3) * 128;
            warm = __builtin_amdgcn_raw_buffer_load_b32(RSRC(ws + WS_STAT, (int)(WS_PART - WS_STAT)), off, 0, AUX_SC1);
        }
    }
    const __amdgpu_buffer_rsrc_t rI = RSRC(idx_in, T4 * 16), rO = RSRC(idx_out, T4 * 16), rA = RSRC(angle, T4 * 8);
    const int rem0 = RF * NWG * NT + c * RQ;
    const bool valid0 = tid < min(RQ, T4 - rem0);
    auto skip = [&](int i) -> bool { return i == 0 ? w * 64 >= RQ : (!FULL && i - 1 >= RF); };
    const int mper = (m_n16 + NWG - 1) / NWG;
    const int m0i = min(c * mper, m_n16);
    const __amdgpu_buffer_rsrc_t rT = RSRC(ws + WS_OFFS, (int)(WS_FLAG - WS_OFFS));

    u32x4 rin[NQ], rout[NQ];
    u32x2 rang[NQ];
    u32 P[NQ][4], B8[NQ], BK[NQ][2];

    const u32 t16 = tid * 16, t8 = tid * 8;
    const u32 epoch = __builtin_amdgcn_raw_buffer_load_b32(rT, (int)(WS_EPOCH - WS_OFFS), 0, AUX_SC1);
    const u32x4 mv = __builtin_amdgcn_raw_buffer_load_b128(RSRC(m16 + m0i, min(mper, m_n16 - m0i) * 16), t16, 0, 0);
    auto issue = [&](int i) {
        if (i == 0) {
            const u32 q = valid0 ? rem0 + tid : 0x0fffffffu;
            rin[i] = __builtin_amdgcn_raw_buffer_load_b128(rI, q * 16, 0, AUX_NT);
            rout[i] = __builtin_amdgcn_raw_buffer_load_b128(rO, q * 16, 0, AUX_NT);
            rang[i] = __builtin_amdgcn_raw_buffer_load_b64(rA, q * 8, 0, AUX_NT);
        } else if (!skip(i)) {
            const u32 qb = ((i - 1) * NWG + c) * NT;
            rin[i] = __builtin_amdgcn_raw_buffer_load_b128(rI, t16, qb * 16, AUX_NT);
            rout[i] = __builtin_amdgcn_raw_buffer_load_b128(rO, t16, qb * 16, AUX_NT);
            rang[i] = __builtin_amdgcn_raw_buffer_load_b64(rA, t8, qb * 8, AUX_NT);
        }
    };
    issue(0); issue(1);
    const float freq = bf2f(ang_freq[0]);
    SCHED();
    const u32x4 z4 = {0, 0, 0, 0};
#pragma unroll
    for (int k = 0; k < LDS_BYTES / 16 / NT; k++) *LP(u32x4, t16 + k * NT * 16) = z4;
    __syncthreads();
    asm volatile("" :: "v"(warm));
    const u32 tag = __builtin_amdgcn_readfirstlane(epoch) + 0x9e3779b9u;
    {
        u32 lo[4], hi[4], mx = 0;
#pragma unroll
        for (int e = 0; e < 4; e++) { lo[e] = (mv[e] << 16) & 0x7fffffffu; hi[e] = mv[e] & 0x7fff0000u; mx = max(mx, max(lo[e], hi[e])); }
        mx = wave_incl_max(mx);
        f32x4 sa = {__uint_as_float(lo[0]) + __uint_as_float(lo[2]), __uint_as_float(hi[0]) + __uint_as_float(hi[2]),
                    __uint_as_float(lo[1]) + __uint_as_float(lo[3]), __uint_as_float(hi[1]) + __uint_as_float(hi[3])};
#pragma unroll
        for (int e = 0; e < 4; e++) sa[e] = wave_incl_sumf(sa[e]);
        if (lane == 63) {
            __hip_atomic_fetch_max(LP(u32, P1_SCR + 64), mx, __ATOMIC_RELAXED, __HIP_MEMORY_SCOPE_WORKGROUP);
            *LP(float, P1_SCR + 128 + w * 4) = fmaxf(fmaxf(sa[0], sa[1]), fmaxf(sa[2], sa[3]));
        }
    }
    const bool unit_freq = __builtin_amdgcn_readfirstlane(__float_as_uint(freq)) == 0x3f800000u;
    u32 nanacc = 0;
    u32 vm8 = 0x80008000u, vmt = 0xf8000000u, v12 = 12;
    PIN(vm8); PIN(vmt); PIN(v12);

    auto encode = [&](int i) {
        if (skip(i)) return;
        u32 x01 = rang[i].x, x23 = rang[i].y;
        if (!unit_freq) {
            x01 = pk_bf16(half_of(x01, 0) * freq, half_of(x01, 1) * freq);
            x23 = pk_bf16(half_of(x23, 0) * freq, half_of(x23, 1) * freq);
        }
        const float x[4] = {half_of(x01, 0), half_of(x01, 1), half_of(x23, 0), half_of(x23, 1)};
        float r[4];
#pragma unroll
        for (int k = 0; k < 4; k++) r[k] = x[k] * 0.15915494309189535f;
        const float rm = fmaxf(fmaxf(fabsf(r[0]), fabsf(r[1])), fmaxf(fabsf(r[2]), fabsf(r[3])));
        if (__builtin_expect(__any(rm >= 20.0f), 0)) {
#pragma unroll
            for (int k = 0; k < 4; k++) r[k] = fabsf(x[k]) >= 128.0f ? rev_large(x[k]) : r[k];
        }
        const u32 c01 = pk_bf16(__builtin_amdgcn_cosf(r[0]), __builtin_amdgcn_cosf(r[1]));
        const u32 c23 = pk_bf16(__builtin_amdgcn_cosf(r[2]), __builtin_amdgcn_cosf(r[3]));
        nanacc = or3(c01, c23, nanacc);
        PIN(nanacc);
        B8[i] = __builtin_amdgcn_perm(c23, c01, 0x06040200u);
        const u32 t01 = bsel(vm8, c01, c01 << 3), t23 = bsel(vm8, c23, c23 << 3);
        const u32 F[4] = {t01 << 16, t01, t23 << 16, t23};
        u32 bk[4];
#pragma unroll
        for (int k = 0; k < 4; k++) {
            const u32 vi = rin[i][k], vo = rout[i][k];
            const u32 q = mulhi24(vi, magI);
            const u32 p = mulhi24(vo, magW);
            const u32 li = mad24(q, -(int)WI, vi);
            const u32 lo = mad24(p, -(int)W, vo);
            P[i][k] = bsel(vmt, F[k], ((lo << 13) + li) ^ 0x07ffe000u);
            bk[k] = shl2_16((q << 5) + p);
            if (i == 0) bk[k] = valid0 ? bk[k] : (u32)(P1_DUMMY + lane * 4);
            lds_add_rtn(bk[k], i < 7 ? 1u : 0x10000u);
        }
        BK[i][0] = bk[0] | (bk[1] << 16);
        BK[i][1] = bk[2] | (bk[3] << 16);
        PIN(BK[i][0]); PIN(BK[i][1]); PIN(B8[i]); PIN(P[i][0]); PIN(P[i][1]); PIN(P[i][2]); PIN(P[i][3]);
    };
    const __amdgpu_buffer_rsrc_t rR = RSRC(ws + WS_REC, NSUB * UPC * 16);
    auto scan = [&](int sub) -> u32 {
        u32 un[7], st[7];
        if (w < 4) {
#pragma unroll
            for (int k = 0; k < 7; k++) un[k] = div3((*LP(u32, tid * 4 + k * 1024) >> (16 * sub)) + 2);
            u32 run = 0;
#pragma unroll
            for (int j = 0; j < 4; j++) {
                const u32 v = half_incl_sum(un[2 * j] | (j < 3 ? un[2 * j + 1] << 16 : 0u));
                const u32 t0 = __builtin_amdgcn_readlane(v, 31), t1 = __builtin_amdgcn_readlane(v, 63);
                const u32 tt = lane < 32 ? t0 : t1;
                st[2 * j] = run + (v & 0xffffu) - un[2 * j];
                run += tt & 0xffffu;
                if (j < 3) { st[2 * j + 1] = run + (v >> 16) - un[2 * j + 1]; run += tt >> 16; }
            }
            if ((lane & 31) == 0) *LP(u32, P1_SCR + (tid >> 5) * 4) = run;
        }
        if (w == 8) *LP(u32, P1_DUMMY + lane * 4) = 0xffffu;
        __syncthreads();
        const u32 ht = *LP(u32, P1_SCR + (lane & 7) * 4);
        u32 hs = ht + dpp0<0x111, 0xf>(ht);
        hs += dpp0<0x112, 0xf>(hs); hs += dpp0<0x114, 0xf>(hs);
        const u32 U = __builtin_amdgcn_readlane(hs, 7);
        if (w < 4) {
            const u32 b0 = __builtin_amdgcn_readlane(hs - ht, 2 * w), b1 = __builtin_amdgcn_readlane(hs - ht, 2 * w + 1);
            const u32 base = lane < 32 ? b0 : b1;
            u32 nn[7], dd[7], sat = *LP(u32, P1_SCR + 96);
#pragma unroll
            for (int k = 0; k < 7; k++) {
                st[k] += base;
                *LP(u32, tid * 4 + k * 1024) = st[k] * 3;
                nn[k] = min(un[k], 31u);
                dd[k] = k ? min(st[k] - st[k - 1], 1023u) : 0u;
                sat |= (nn[k] == 31u) | (k != 4 && dd[k] == 1023u);
            }
            if (sat) nn[0] = 31u;
            const u32 x0 = st[0] | (dd[1] << 14) | (nn[0] << 24) | (nn[3] << 29), z0 = dd[2] | (dd[3] << 10) | (nn[1] << 20) | (nn[2] << 25) | ((nn[3] >> 3) << 30);
            const u32 x1 = st[4] | (dd[5] << 14) | (nn[4] << 24), z1 = dd[6] | (nn[5] << 20) | (nn[6] << 25);
#pragma unroll
            for (int j = 0; j < 2; j++) {
                const int src = (j * 32 + (lane >> 1)) * 4;
                const u32 a0 = __builtin_amdgcn_ds_bpermute(src, x0), a1 = __builtin_amdgcn_ds_bpermute(src, x1);
                const u32 c0 = __builtin_amdgcn_ds_bpermute(src, z0), c1 = __builtin_amdgcn_ds_bpermute(src, z1);
                const u32x4 o = {(lane & 1) ? a1 : a0, tag, (lane & 1) ? c1 : c0, tag};
                __builtin_amdgcn_raw_buffer_store_b128(o, rT, (sub * NWG + c) * 8192 + w * 2048 + j * 1024 + lane * 16, 0, AUX_SC1);
            }
            if (sat) {
#pragma unroll
                for (int k = 0; k < 7; k++)
                    __builtin_amdgcn_raw_buffer_store_b32(st[k] | (un[k] << 16), rT, (int)(WS_OFFX - WS_OFFS) + (sub * NWG + c) * 8192 + tid * 32 + k * 4, 0, AUX_SC1);
            }
        }
        if (U > (u32)P1_UA) {
            for (u32 u = P1_UA + tid; u < U; u += NT) __builtin_amdgcn_raw_buffer_store_b128(z4, rR, ((sub * NWG + c) * UPC + u) * 16, 0, AUX_SC1);
            asm volatile("s_waitcnt vmcnt(0)" ::: "memory");
        }
        __syncthreads();
        return U;
    };
    u32 pos[NQ][4];
    auto cursor = [&](int i) {
        if (skip(i)) return;
        PIN(BK[i][0]); PIN(BK[i][1]);
        const u32 inc = i > 0 || valid0 ? 1u : 0u;
        pos[i][0] = lds_add_rtn(BK[i][0] & 0xffffu, inc); pos[i][1] = lds_add_rtn(BK[i][0] >> 16, inc);
        pos[i][2] = lds_add_rtn(BK[i][1] & 0xffffu, inc); pos[i][3] = lds_add_rtn(BK[i][1] >> 16, inc);
    };
    auto scatter = [&](int i, bool big) {
        if (skip(i)) return;
#pragma unroll
        for (int k = 0; k < 4; k++) {
            const u32 ps = i < 7 ? (pos[i][k] & 0xffffu) : pos[i][k];
            const u32 t = div3(ps), zz = t >> 1;
            *LP(u32, __builtin_amdgcn_bitop3_b32((ps + t) << 2, zz, v12, 0x78) + P1_STG) = P[i][k];
            const u32 src = (k & 1) ? (B8[i] >> 8) : B8[i];
            *LP(unsigned char, __builtin_amdgcn_bitop3_b32(mad24(t, 13, ps), zz, v12, 0xf2) + P1_STG) = (unsigned char)((k & 2) ? (src >> 16) : src);
        }
        if (big) {
            const u32 g = ((i >= 7) * NWG + c) * UPC * 16;
#pragma unroll
            for (int k = 0; k < 4; k++) {
                const u32 ps = pos[i][k] & 0xffffu, t = div3(ps);
                if (t >= (u32)P1_UA && ps != 0xffffu) {
                    __builtin_amdgcn_raw_buffer_store_b32(P[i][k], rR, g + (ps + t) * 4, 0, AUX_SC1);
                    __builtin_amdgcn_raw_buffer_store_b8((unsigned char)(B8[i] >> (8 * k)), rR, g + 13 * t + ps + 12, 0, AUX_SC1);
                }
            }
        }
    };
    const u32 sw1 = (tid & 8) ? ~0u : 0u, sw2 = (tid & 16) ? ~0u : 0u;
    auto unswz = [&](u32x4 v) -> u32x4 {
        const u32x4 a = {bsel(sw1, v.y, v.x), bsel(sw1, v.x, v.y), bsel(sw1, v.w, v.z), bsel(sw1, v.z, v.w)};
        return u32x4{bsel(sw2, a.z, a.x), bsel(sw2, a.w, a.y), bsel(sw2, a.x, a.z), bsel(sw2, a.y, a.w)};
    };

    auto piece = [&](int k) -> u32 {
        u32 r;
        asm volatile("v_add_u32 %0, %1, %2" : "=v"(r) : "i"(k * NT * 16), "v"(t16));
        return r;
    };

#pragma unroll
    for (int i = 0; i < 7; i++) {
        issue(i + 2);
        SCHED();
        encode(i);
        SCHED();
    }
    __syncthreads();
    const u32 UA_ = scan(0);
    if (tid == 0) {
        float t = 0.f;
#pragma unroll
        for (int k = 0; k < 4; k++) { const f32x4 v = *LP(f32x4, P1_SCR + 128 + k * 16); t += (v.x + v.y) + (v.z + v.w); }
        __builtin_amdgcn_raw_buffer_store_b128(u32x4{*LP(u32, P1_SCR + 64), tag, __float_as_uint(t), tag}, rT, (int)(WS_STAT - WS_OFFS) + c * 16, 0, AUX_SC1);
    }
    {
        const bool big = UA_ > (u32)P1_UA;
        cursor(0);
#pragma unroll
        for (int i = 0; i < 7; i++) {
            if (i + 1 < 7) cursor(i + 1);
            SCHED();
            scatter(i, big);
            SCHED();
            if (i & 1) encode(7 + i / 2);
            else issue(9 + i / 2);
            SCHED();
        }
    }
    __syncthreads();
    u32x4 DR[ND];
#pragma unroll
    for (int k = 0; k < ND; k++) {
        DR[k] = *LP(u32x4, P1_STG + t16 + k * NT * 16);
        *LP(u32x4, P1_STG + t16 + k * NT * 16) = z4;
    }
    SCHED();
    const __amdgpu_buffer_rsrc_t rS0 = RSRC(ws + WS_REC + (size_t)(u32)(c * UPC) * 16u, min((UA_ + 7) & ~7u, (u32)P1_UA) * 16);
    const int h = c & 7, p = c >> 3;
    const int task = h * 32 + p;
    const bool sg = lane >= 32;
    const int cc = w * 32 + (lane & 31);
    u32x4 o4 = {0, 0, 0, 0}, mxv = {0, tag, 0, tag};
    auto load_tables = [&]() {
        o4 = __builtin_amdgcn_raw_buffer_load_b128(rT, cc * 8192 + task * 32 + (sg ? 16 : 0), 0, AUX_SC1);
        if (w < 4) mxv = __builtin_amdgcn_raw_buffer_load_b128(rT, (int)(WS_STAT - WS_OFFS) + t16, 0, AUX_SC1);
        SCHED();
    };
    encode(10); SCHED();
    encode(11); SCHED();
    encode(12); SCHED();
    if (nanacc & 0x40004000u) *LP(u32, P1_SCR + 96) = 1;
    __syncthreads();
    const u32 UB_ = scan(1);
    if (FULL && w < 8) load_tables();
    {
        const bool big = UB_ > (u32)P1_UA;
        cursor(7);
#pragma unroll
        for (int i = 7; i < NQ; i++) {
            if (i + 1 < NQ) cursor(i + 1);
            SCHED();
#pragma unroll
            for (int k = 2 * (i - 8); k < 2 * (i - 7); k++)
                if (k >= 0) { __builtin_amdgcn_raw_buffer_store_b128(unswz(DR[k]), rS0, piece(k), 0, AUX_SC1); SCHED(); }
            scatter(i, big);
            SCHED();
        }
    }
    __syncthreads();

#pragma unroll
    for (int k = 0; k < ND; k++) DR[k] = *LP(u32x4, P1_STG + t16 + k * NT * 16);
    const __amdgpu_buffer_rsrc_t rS1 = RSRC(ws + WS_REC + (size_t)(u32)((NWG + c) * UPC) * 16u, min((UB_ + 7) & ~7u, (u32)P1_UA) * 16);
    auto stores = [&](int k0, int k1) {
#pragma unroll
        for (int k = k0; k < k1; k++) { __builtin_amdgcn_raw_buffer_store_b128(unswz(DR[k]), rS1, piece(k), 0, AUX_SC1); SCHED(); }
    };
    auto arrive = [&](int ad) { asm volatile("" ::: "memory"); SCHED(); if (lane == 0) lds_add_rtn(ad, 1u); SCHED(); };
    auto await = [&](int ad) {
        SCHED();
        while (__builtin_amdgcn_readfirstlane(__hip_atomic_load(LP(u32, ad), __ATOMIC_RELAXED, __HIP_MEMORY_SCOPE_WORKGROUP)) < 16u)
            __builtin_amdgcn_s_sleep(2);
        asm volatile("" ::: "memory");
        SCHED();
    };
    if (!FULL || w >= 8) load_tables();
    stores(0, 1);
    __syncthreads();
#pragma unroll
    for (int k = 0; k < 5; k++) *LP(u32x4, P2_HEAD + t16 + k * NT * 16) = z4;
    if (w < 4) *LP(u32x4, P2_HEAD + t16 + 5 * NT * 16) = z4;
    if (tid == 0) *LP(u32, P2_SCR + 400) = 0;
    if (tid < 16) *LP(u32, P2_SCR + 448 + tid * 4) = 0;
    SCHED();
#pragma unroll 1
    for (u32 spins = 0; spins < (1u << 20) && !__all(o4.y == tag && o4.w == tag && mxv.y == tag && mxv.w == tag); spins++) {
        asm volatile("" ::: "memory");
        load_tables();
    }
    u32 n[4], inc[4], st[4];
    n[0] = (o4.x >> 24) & 31u; n[1] = (o4.z >> 20) & 31u; n[2] = (o4.z >> 25) & 31u; n[3] = (o4.x >> 29) | ((o4.z >> 30) << 3);
    st[0] = o4.x & 0x3fffu; st[1] = st[0] + ((o4.x >> 14) & 1023u); st[2] = st[1] + (o4.z & 1023u); st[3] = st[2] + ((o4.z >> 10) & 1023u);
#pragma unroll
    for (int k = 0; k < 4; k++) inc[k] = half_incl_sum(k == 0 && n[0] == 31u ? (u32)LCAP + 1 : n[k]);
    if ((lane & 31) == 31) {
#pragma unroll
        for (int k = 0; k < 4; k++) *LP(u32, P2_SCR + 512 + ((sg * 4 + k) * 16 + w) * 4) = inc[k];
    }
    if (w < 4) {
        const u32 mx = wave_incl_max(mxv.x);
        const float sm = wave_incl_sumf(__uint_as_float(mxv.z));
        if (lane == 63) { *LP(u32, P2_SCR + 384 + w * 4) = mx; *LP(float, P2_SCR + 416 + w * 4) = sm; }
    }
    arrive(P1_SCR + 72);
    stores(1, 4);
    await(P1_SCR + 72);
    u64 m3;
    bool slow;
    {
        const u32 ta = *LP(u32, P2_SCR + 512 + lane * 4), tb = *LP(u32, P2_SCR + 768 + lane * 4);
        u32 ia = row_incl_sum(ta), ib = row_incl_sum(tb);
        slow = __any(max(ia, ib) > (u32)LCAP);
        m3 = (__ballot(ia > (u32)(2 * NT + w * 64)) & 0x8000800080008000ull) | ((__ballot(ib > (u32)(2 * NT + w * 64)) & 0x8000800080008000ull) >> 1);
        const u32 tt = __builtin_amdgcn_readlane(w < 4 ? ia : ib, (w & 3) * 16 + 15);
        ia -= ta; ib -= tb;
        bool more = false;
        u32 nx[4], en[4], dl[4];
        const u32 hb0 = P2_HEAD + (sg ? 4 * LCAP * 4 : 0);
#pragma unroll
        for (int k = 0; k < 4; k++) {
            const u32 ca = __builtin_amdgcn_readlane(ia, k * 16 + w), cb = __builtin_amdgcn_readlane(ib, k * 16 + w);
            const u32 carry = sg ? cb : ca;
            const u32 ex = carry + inc[k] - n[k];
            en[k] = min(carry + inc[k], (u32)LCAP);
            dl[k] = ((u32)cc * UPC + st[k] - ex) * 16;
            const u32 hb = hb0 + k * LCAP * 4;
            *LP(u32, ex < en[k] ? hb + ex * 4 : LDS_BYTES) = dl[k];
            const u32 b1 = (ex | 7u) + 1;
            *LP(u32, b1 < en[k] ? hb + b1 * 4 : LDS_BYTES) = dl[k];
            nx[k] = b1 + 8;
            more |= nx[k] < en[k];
        }
        if (more) {
#pragma unroll
            for (int k = 0; k < 4; k++)
                for (u32 ps = nx[k]; ps < en[k]; ps += 8) *LP(u32, hb0 + k * LCAP * 4 + ps * 4) = dl[k];
        }
        if (w < NS) {
            for (u32 ps = (tt & ~7u) + lane * 8; ps < (u32)LCAP; ps += 512) *LP(u32, P2_HEAD + (w * LCAP + max(ps, tt)) * 4) = 0xf0000000u;
        }
    }
    const u32x4 mq = *LP(u32x4, P2_SCR + 384);
    const u32 maxbits = __builtin_amdgcn_readfirstlane(max(max(mq[0], mq[1]), max(mq[2], mq[3])));
    const f32x4 sq = *LP(f32x4, P2_SCR + 416);
    const float rtyp = rfac * ((sq.x + sq.y) + (sq.z + sq.w));
    const float range = fmaxf(rtyp, 2.002f * __uint_as_float(maxbits));
    slow = slow || maxbits < 0x10000000u || maxbits > 0x70000000u || __any(range > 2.0f * rtyp);
    arrive(P1_SCR + 76);
    stores(4, 7);
    await(P1_SCR + 76);
    u32 hv[NS][3];
#pragma unroll
    for (int s = 0; s < NS; s++)
#pragma unroll
        for (int t = 0; t < 3; t++) hv[s][t] = *LP(u32, P2_HEAD + (s * LCAP + t * NT + tid) * 4);
    stores(7, ND);
    asm volatile("s_waitcnt vmcnt(0) lgkmcnt(0)\n\ts_barrier" ::: "memory");
    if (w == 14 && lane < 8) __builtin_amdgcn_raw_buffer_store_b32(tag, rT, (int)(WS_PHASE - WS_OFFS) + lane * 1024 + c * 4, 0, AUX_SC1);
    const __amdgpu_buffer_rsrc_t rM = RSRC(m16, m_n16 * 16);
    u32x4 wr[NS][3]; u32x2 wr3[NS];
    auto wload = [&](int j) {
        const int s = j >> 2, k = j & 3;
        const u32 base = (u32)(s * NH + h) * WI * 8;
        if (k < 3) wr[s][k] = __builtin_amdgcn_raw_buffer_load_b128(rM, base + k * 16384 + t16, 0, 0);
        else wr3[s] = __builtin_amdgcn_raw_buffer_load_b64(rM, base + 49152 + t8, 0, 0);
    };
    auto wstore = [&](int s) {
#pragma unroll
        for (int k = 0; k < 3; k++) *LP(u32x4, k * 16384 + t16) = wr[s][k];
        *LP(u32x2, 49152 + t8) = wr3[s];
    };
#pragma unroll
    for (int j = 0; j < 4; j++) wload(j);
    SCHED();
    {
        const u32x4 b4 = {ABIAS, ABIAS, ABIAS, ABIAS};
#pragma unroll
        for (int k = 0; k < 6; k++) *LP(u32x4, P2_ACC + t16 + k * NT * 16) = b4;
        if (w < 2) *LP(u32x4, P2_ACC + t16 + 6 * NT * 16) = b4;
    }
    u32 p16[3] = {t16, t16 + NT * 16, t16 + 2 * NT * 16};
    PIN(p16[1]); PIN(p16[2]);
    auto expand = [&](int s) {
#pragma unroll
        for (int t = 0; t < 3; t++) {
            u32 v = hv[s][t];
            v = max(v, dpp0<0x111, 0xf>(v));
            v = max(v, dpp0<0x112, 0xf>(v));
            v = max(v, dpp0<0x114, 0xf>(v));
            hv[s][t] = v + p16[t];
        }
#pragma unroll
        for (int t = 0; t < 3; t++) PIN(hv[s][t]);
    };
    expand(0); expand(1);
    SCHED();
    wstore(0);
    asm volatile("" :: "s"(centers[0]), "s"(w_sbf[0]), "s"(w_rbf[0]), "s"(w_down[0]), "s"(w_up[0]));

    const float quantum = __uint_as_float(__builtin_amdgcn_readfirstlane(__float_as_uint(range * (1.0f / AB16))));
    u32 ovf = 0;
    auto handoff = [&]() {
        if (w == 15) {
#pragma unroll 1
            for (u32 spins = 0; spins < (1u << 20); spins++) {
                asm volatile("" ::: "memory");
                const u32x4 v = __builtin_amdgcn_raw_buffer_load_b128(rT, (int)(WS_PHASE - WS_OFFS) + h * 1024 + lane * 16, 0, AUX_SC1);
                if (__all(v.x == tag && v.y == tag && v.z == tag && v.w == tag)) break;
                __builtin_amdgcn_s_sleep(1);
            }
        }
        asm volatile("s_waitcnt lgkmcnt(0)\n\ts_barrier" ::: "memory");
        if (c == 0 && tid == 0) __builtin_amdgcn_raw_buffer_store_b32(tag, rT, (int)(WS_EPOCH - WS_OFFS), 0, AUX_SC1);
    };
    if (!slow) {
        float S = (float)AB16 / range;
        float MAG = 8421376.0f;
        u32 vmf = 0x8f000000u, v30 = 0x30000000u, vma = 0x1fff8u;
        PIN(MAG); PIN(vmf); PIN(v30); PIN(S); PIN(vma);
        auto third = [&](int s) -> bool { return (m3 >> (16 * (s & 3) + 15 - (s >> 2))) & 1; };
        u32x4 RP[NS][3];
        u32x2 G[NS][9];
        u64 OLD[NS][9];
        constexpr int wsch[NS + 1] = {16, 18, 20, 23, 25, 28, 28, 28};
#pragma unroll
        for (int j = 4; j < wsch[0]; j++) wload(j);
        SCHED();
        handoff();
        auto rload = [&](int s, int t) {
            if (s < NS && (t < 2 || third(s))) RP[s][t] = __builtin_amdgcn_raw_buffer_load_b128(rR, hv[s][t], 0, AUX_SC1);
            SCHED();
        };
        auto gather = [&](int s, int t) {
#pragma unroll
            for (int k = 0; k < 3; k++) G[s][3 * t + k] = *LP(u32x2, shl3_16(RP[s][t][k]));
        };
        auto accum = [&](int s, int r) {
            const int t = r / 3, k = r % 3;
            const u32 Pk = RP[s][t][k];
            const u32 lowb = __builtin_amdgcn_perm(v30, RP[s][t][3], 0x07000c0cu | ((u32)k << 16));
            const float cs = __uint_as_float(andor((u32)((int)Pk >> 3), vmf, lowb)) * S;
            const u32 x0 = __float_as_uint(__builtin_fmaf(half_of(G[s][r].x, 0), cs, MAG));
            const u32 x1 = __float_as_uint(__builtin_fmaf(half_of(G[s][r].x, 1), cs, MAG));
            const u32 x2 = __float_as_uint(__builtin_fmaf(half_of(G[s][r].y, 0), cs, MAG));
            const u32 x3 = __float_as_uint(__builtin_fmaf(half_of(G[s][r].y, 1), cs, MAG));
            const u32x2 pa = {__builtin_amdgcn_perm(x1, x0, 0x05040100u), __builtin_amdgcn_perm(x3, x2, 0x05040100u)};
            const u64 a = __builtin_bit_cast(u64, pa) - 0x8000800080008000ull;
            const u32 ad = __builtin_amdgcn_bitop3_b32(Pk >> 10, vma, vma, 0x08);
            OLD[s][r] = __hip_atomic_fetch_add(LP(u64, ad + P2_ACC), a, __ATOMIC_RELAXED, __HIP_MEMORY_SCOPE_WORKGROUP);
            SCHED();
        };
        auto check = [&](int s, int r) {
            const u32x2 y = __builtin_bit_cast(u32x2, OLD[s][r]);
            ovf = or3(y.x, y.y, ovf);
            PIN(ovf);
        };
        rload(0, 0); rload(0, 1); rload(0, 2);
#pragma unroll
        for (int s = 2; s < NS; s++) expand(s);
#pragma unroll
        for (int s = 0; s < NS; s++) {
            SCHED();
            if (s == 0) {
                gather(0, 0); SCHED();
                rload(1, 2); rload(1, 0);
                accum(0, 0); accum(0, 1); accum(0, 2);
                gather(0, 1); SCHED();
                rload(1, 1);
                if (third(0)) gather(0, 2);
                arrive(P2_SCR + 448);
            } else {
                await(P2_SCR + 448 + (2 * s - 1) * 4);
                if (third(s)) gather(s, 2);
                gather(s, 0); gather(s, 1);
                if (s + 1 < NS) arrive(P2_SCR + 448 + 2 * s * 4);
                rload(s + 1, 1);
                check(s - 1, 0); check(s - 1, 1);
                accum(s - 1, 3); accum(s - 1, 4); accum(s - 1, 5);
                check(s - 1, 2);
            }
            if (s + 1 < NS) {
                await(P2_SCR + 448 + 2 * s * 4);
                wstore(s + 1);
                arrive(P2_SCR + 448 + (2 * s + 1) * 4);
            }
            const int wn = wsch[s + 1] - wsch[s];
            if (wn > 0) { wload(wsch[s]); SCHED(); }
            if (s > 0) { check(s - 1, 3); check(s - 1, 4); check(s - 1, 5); }
            if (third(s)) {
                accum(s, 6); accum(s, 7); accum(s, 8);
                check(s, 6); check(s, 7); check(s, 8);
                SCHED();
            }
            rload(s + 2, 2);
            if (wn > 2) { wload(wsch[s] + 1); SCHED(); }
            if (s > 0) accum(s, 0);
            if (wn > 0) { wload(wsch[s + 1] - 1); SCHED(); }
            rload(s + 2, 0);
            if (s > 0) { accum(s, 1); accum(s, 2); }
        }
        accum(NS - 1, 3); accum(NS - 1, 4); accum(NS - 1, 5);
#pragma unroll
        for (int r = 0; r < 6; r++) check(NS - 1, r);
    } else {
        handoff();
    }
    const u32 e0 = (u32)p * W + (u32)h * WQ;
    const u32 ne = min(min((u32)WQ, W - min(W, (u32)h * WQ)), (u32)E - min((u32)E, e0));
    const bool act = w * 128 < WQ;
    u32x4 em = {0, 0, 0, 0}; u32 ed = 0;
    if (act) {
        em = __builtin_amdgcn_raw_buffer_load_b128(rM, (e0 + tid * 2) * 8, 0, 0);
        ed = __builtin_amdgcn_raw_buffer_load_b32(RSRC(dist, E * 2), (e0 + tid * 2) * 2, 0, 0);
    }
    if (ovf & 0x80008000u) *LP(u32, P2_SCR + 400) = 1;
    __syncthreads();
    slow = slow || __builtin_amdgcn_readfirstlane(*LP(u32, P2_SCR + 400)) != 0;

    u32 cw[4], wsb[16], wrb[16], wdn[8], wup[8];
#pragma unroll
    for (int k = 0; k < 4; k++) cw[k] = centers[k];
#pragma unroll
    for (int k = 0; k < 16; k++) { wsb[k] = w_sbf[k]; wrb[k] = w_rbf[k]; }
#pragma unroll
    for (int k = 0; k < 8; k++) { wdn[k] = w_down[k]; wup[k] = w_up[k]; }
    const __amdgpu_buffer_rsrc_t rX = RSRC(ws + WS_PART + (size_t)p * 64 * WQ * 16, 64 * WQ * 16);
    const __amdgpu_buffer_rsrc_t rY = RSRC(ws + WS_PART16 + (size_t)p * 64 * WQ * 8, 64 * WQ * 8);
    u32x4 own = {ABIAS, ABIAS, ABIAS, ABIAS};
    f32x4 sm0 = {0, 0, 0, 0}, sm1 = {0, 0, 0, 0};
    if (!slow) {
        if (tid < WQ / 2) {
#pragma unroll
            for (int q = 1; q < NH; q++) {
                const u32 hd = (h + q) & 7;
                const u32x4 v = *LP(u32x4, P2_ACC + (hd * WQ + tid * 2) * 8);
                __builtin_amdgcn_raw_buffer_store_b128(v, rY, ((hd * 8 + h) * WQ + tid * 2) * 8, 0, AUX_SC1);
            }
            own = *LP(u32x4, P2_ACC + (h * WQ + tid * 2) * 8);
        }
    } else {
        const u16* m2 = (const u16*)m16;
        for (int cmp = 0; cmp < 4; cmp++) {
            __syncthreads();
            for (u32 k = tid; k < MAXW / 2; k += NT) *LP(u32x4, P2_ACC + k * 16) = z4;
            __syncthreads();
            for (int r = w; r < NSUB * NS; r += 16) {
                const int ck = r / NS, s = r - ck * NS;
                const u32 e0x = __builtin_amdgcn_raw_buffer_load_b32(rT, ck * 8192 + task * 32, 0, AUX_SC1);
                const u32x4 oe = __builtin_amdgcn_raw_buffer_load_b128(rT, ck * 8192 + task * 32 + (s >> 2) * 16, 0, AUX_SC1);
                const int k4 = s & 3;
                u32 b0 = (oe.x & 0x3fffu) + (k4 > 0 ? (oe.x >> 14) & 1023u : 0) + (k4 > 1 ? oe.z & 1023u : 0) + (k4 > 2 ? (oe.z >> 10) & 1023u : 0);
                u32 b1 = k4 == 0 ? (oe.x >> 24) & 31u : k4 == 1 ? (oe.z >> 20) & 31u : k4 == 2 ? (oe.z >> 25) & 31u : (oe.x >> 29) | ((oe.z >> 30) << 3);
                if (((e0x >> 24) & 31u) == 31u) {
                    const u32 x = __builtin_amdgcn_raw_buffer_load_b32(rT, (int)(WS_OFFX - WS_OFFS) + ck * 8192 + task * 32 + s * 4, 0, AUX_SC1);
                    b0 = x & 0xffffu; b1 = x >> 16;
                }
                b0 += (u32)ck * UPC; b1 += b0;
                const u32 inb = (u32)(s * NH + h) * WI;
                for (u32 j = b0 + lane; j < b1; j += 64) {
                    const u32x4 un = __builtin_amdgcn_raw_buffer_load_b128(rR, j * 16, 0, AUX_SC1);
#pragma unroll
                    for (int k = 0; k < 3; k++) {
                        const u32 Pk = un[k];
                        if (Pk == 0) continue;
                        u32 F = (((u32)((int)Pk >> 3)) & 0x8f000000u) | 0x30000000u | (((un[3] >> (8 * k)) & 0xffu) << 16);
                        if ((F & 0x7fffffffu) == 0x30000000u) F &= 0x80000000u;
                        if ((F & 0x7fffffffu) > 0x3f800000u) F = 0x7fc00000u;
                        const float mm = bf2f(m2[(size_t)(inb + (Pk & 0x1fffu)) * 4 + cmp]);
                        __hip_atomic_fetch_add(LP(double, P2_ACC + (~(Pk >> 10) & 0x1fff8u)), (double)__uint_as_float(F) * (double)mm,
                                               __ATOMIC_RELAXED, __HIP_MEMORY_SCOPE_WORKGROUP);
                    }
                }
            }
            __syncthreads();
            for (u32 e = tid; e < MAXW; e += NT) {
                const u32 hd = e / WQ, el = e - hd * WQ;
                __builtin_amdgcn_raw_buffer_store_b32(__float_as_uint((float)*LP(double, P2_ACC + e * 8)), rX, ((hd * 8 + h) * WQ + el) * 16 + cmp * 4, 0, AUX_SC1);
            }
            if (tid < WQ / 2) {
                const float a = (float)*LP(double, P2_ACC + (h * WQ + tid * 2) * 8), b = (float)*LP(double, P2_ACC + (h * WQ + tid * 2) * 8 + 8);
                sm0 = f32x4{cmp == 0 ? a : sm0.x, cmp == 1 ? a : sm0.y, cmp == 2 ? a : sm0.z, cmp == 3 ? a : sm0.w};
                sm1 = f32x4{cmp == 0 ? b : sm1.x, cmp == 1 ? b : sm1.y, cmp == 2 ? b : sm1.z, cmp == 3 ? b : sm1.w};
            }
        }
        asm volatile("s_waitcnt vmcnt(0)" ::: "memory");
        __syncthreads();
    }
    if (act) {
        SCHED();
        f32x2 gs[4], hs[4];
        auto gates = [&](int j0, int j1) {
            const f32x2 d = {half_of(ed, 0), half_of(ed, 1)};
#pragma unroll
            for (int j = j0; j < j1; j++) {
                const f32x2 df = rbf16x2(d - half_of(cw[j >> 1], j));
                const f32x2 sq2 = rbf16x2(df * df);
                const f32x2 ar = rbf16x2(sq2 * -10.0f) * 1.4426950408889634f;
                const f32x2 rb = rbf16x2(f32x2{__builtin_amdgcn_exp2f(ar.x), __builtin_amdgcn_exp2f(ar.y)});
#pragma unroll
                for (int k = 0; k < 4; k++) {
                    gs[k] += rb * half_of(wsb[j * 2 + (k >> 1)], k);
                    hs[k] += rb * half_of(wrb[j * 2 + (k >> 1)], k);
                }
            }
        };
#pragma unroll
        for (int k = 0; k < 4; k++) { gs[k] = f32x2{0.f, 0.f}; hs[k] = f32x2{0.f, 0.f}; }
        gates(0, 3);
#pragma unroll
        for (int k = 0; k < 4; k++) { PIN(gs[k]); PIN(hs[k]); }
        asm volatile("s_waitcnt vmcnt(0)" ::: "memory");
        gu32* flags = (gu32*)(ws + WS_FLAG) + (p * 16 + w) * 32;
        if (lane == 0) __hip_atomic_store(flags + h, tag + (slow ? 1u : 0u), __ATOMIC_RELAXED, __HIP_MEMORY_SCOPE_AGENT);
        gates(3, 8);
#pragma unroll
        for (int k = 0; k < 4; k++) { hs[k] = rbf16x2(hs[k]); PIN(gs[k]); PIN(hs[k]); }
#pragma unroll
        for (int k = 0; k < 8; k++) asm volatile("" : "+s"(wdn[k]), "+s"(wup[k]));
        u32 modes;
        {
            gu32* f = flags + ((h + 1 + min(lane, 6)) & 7);
            u32 v = 0;
#pragma unroll 1
            for (u32 spins = 0; spins < (1u << 22); spins++) {
                v = __hip_atomic_load(f, __ATOMIC_RELAXED, __HIP_MEMORY_SCOPE_AGENT) - tag;
                if (__all(v <= 1u)) break;
                __builtin_amdgcn_s_sleep(2);
            }
            modes = (u32)__ballot(v == 1u) & 0x7fu;
            asm volatile("" ::: "memory");
        }
        if (tid * 2 < ne) {
            auto unb = [&](u32x4 v) -> u32x4 {
                const u32x2 a = __builtin_bit_cast(u32x2, __builtin_bit_cast(u64, u32x2{v.x, v.y}) + 0x2000200020002000ull);
                const u32x2 b = __builtin_bit_cast(u32x2, __builtin_bit_cast(u64, u32x2{v.z, v.w}) + 0x2000200020002000ull);
                return u32x4{a.x, a.y, b.x, b.y};
            };
            own = unb(own);
            u32x4 lo = own & 0xffffu, hi = own >> 16;
            if (modes == 0) {
                u32x4 x[NH - 1];
#pragma unroll
                for (int q = 1; q < NH; q++)
                    x[q - 1] = __builtin_amdgcn_raw_buffer_load_b128(rY, ((h * 8 + ((h + q) & 7)) * WQ + tid * 2) * 8, 0, AUX_SC1);
#pragma unroll
                for (int q = 0; q < NH - 1; q++) { const u32x4 y = unb(x[q]); lo += y & 0xffffu; hi += y >> 16; }
            } else {
                for (int q = 1; q < NH; q++) {
                    const u32 idx = (h * 8 + ((h + q) & 7)) * WQ + tid * 2;
                    u32x4 x = {ABIAS, ABIAS, ABIAS, ABIAS};
                    if ((modes >> (q - 1)) & 1) {
                        sm0 += __builtin_bit_cast(f32x4, __builtin_amdgcn_raw_buffer_load_b128(rX, idx * 16, 0, AUX_SC1));
                        sm1 += __builtin_bit_cast(f32x4, __builtin_amdgcn_raw_buffer_load_b128(rX, idx * 16 + 16, 0, AUX_SC1));
                    } else x = __builtin_amdgcn_raw_buffer_load_b128(rY, idx * 8, 0, AUX_SC1);
                    x = unb(x);
                    lo += x & 0xffffu; hi += x >> 16;
                }
            }
            lo -= NH * (AB16 + 0x2000u); hi -= NH * (AB16 + 0x2000u);
            if (modes != 0x7f || !slow) {
                sm0 += f32x4{(float)(int)lo.x, (float)(int)hi.x, (float)(int)lo.y, (float)(int)hi.y} * quantum;
                sm1 += f32x4{(float)(int)lo.z, (float)(int)hi.z, (float)(int)lo.w, (float)(int)hi.w} * quantum;
            }
            f32x2 agg[4], o[4];
#pragma unroll
            for (int k = 0; k < 4; k++) {
                f32x2 v = {0.f, 0.f};
#pragma unroll
                for (int j = 0; j < 4; j++) v += f32x2{sm0[j], sm1[j]} * half_of(wdn[j * 2 + (k >> 1)], k);
                agg[k] = rbf16x2(v * gs[k]);
            }
#pragma unroll
            for (int k = 0; k < 4; k++) {
                f32x2 v = {0.f, 0.f};
#pragma unroll
                for (int j = 0; j < 4; j++) v += agg[j] * half_of(wup[j * 2 + (k >> 1)], k);
                o[k] = f32x2{half_of(em[k >> 1], k), half_of(em[2 + (k >> 1)], k)} + rbf16x2(rbf16x2(v) * hs[k]);
            }
            const u32x4 ov = {pk_bf16(o[0].x, o[1].x), pk_bf16(o[2].x, o[3].x), pk_bf16(o[0].y, o[1].y), pk_bf16(o[2].y, o[3].y)};
            __builtin_amdgcn_raw_buffer_store_b128(ov, RSRC(out, E * 8), (e0 + tid * 2) * 8, 0, 17);
        }
    }
    asm volatile(".Lke%c0:" :: "i"((int)FULL));
}

__global__ void __launch_bounds__(256)
gemnet_scatter_kernel(const u16* __restrict__ m, const int* __restrict__ idx_in, const int* __restrict__ idx_out,
                      const u16* __restrict__ angle, const u16* __restrict__ ang_freq, float* acc, int T, int De, int L)
{
    const long long g = (long long)blockIdx.x * 256 + threadIdx.x;
    const int t = (int)(g / De), k = (int)(g - (long long)t * De);
    if (t >= T) return;
    const float mv = bf2f(m[(size_t)idx_in[t] * De + k]), a = bf2f(angle[t]);
    for (int l = 0; l < L; l++)
        atomicAdd(&acc[((size_t)idx_out[t] * L + l) * De + k], rbf16(cosf(rbf16(bf2f(ang_freq[l]) * a))) * mv);
}
__global__ void __launch_bounds__(256)
gemnet_edge_kernel(const u16* __restrict__ m, const u16* __restrict__ dist, const u16* __restrict__ centers,
                   const u16* __restrict__ w_rbf, const u16* __restrict__ w_sbf, const u16* __restrict__ w_down,
                   const u16* __restrict__ w_up, const float* __restrict__ acc, u16* __restrict__ out, int E, int De, int NR, int L)
{
    const long long g = (long long)blockIdx.x * 256 + threadIdx.x;
    const int e = (int)(g / De), k = (int)(g - (long long)e * De);
    if (e >= E) return;
    const float d = bf2f(dist[e]);
    auto rbf = [&](int r) { const float df = rbf16(d - bf2f(centers[r])); return rbf16(expf(rbf16(-10.0f * rbf16(df * df)))); };
    float hk = 0.f, up = 0.f;
    for (int r = 0; r < NR; r++) hk += rbf(r) * bf2f(w_rbf[r * De + k]);
    for (int j = 0; j < De; j++) {
        float agg = 0.f;
        for (int l = 0; l < L; l++) {
            float gl = 0.f, v = 0.f;
            for (int r = 0; r < NR; r++) gl += rbf(r) * bf2f(w_sbf[(r * L + l) * De + j]);
            for (int i = 0; i < De; i++) v += acc[((size_t)e * L + l) * De + i] * bf2f(w_down[i * De + j]);
            agg += v * gl;
        }
        up += rbf16(agg) * bf2f(w_up[j * De + k]);
    }
    const float o = bf2f(m[(size_t)e * De + k]) + rbf16(rbf16(up) * rbf16(hk));
    out[(size_t)e * De + k] = (u16)pk_bf16(o, o);
}

extern "C" void kernel_launch(void* const* d_in, const int* in_sizes, int n_in,
                              void* d_out, int out_size, void* d_ws, size_t ws_size,
                              hipStream_t stream) {
    const int E = in_sizes[3], T = in_sizes[1], NR = in_sizes[5], L = in_sizes[6];
    const int De = in_sizes[0] / E;
    const u32 W = (((u32)E + 31) / 32 + 1) & ~1u;
    const u32 WI = (((u32)E + NH * NS - 1) / (NH * NS) + 1) & ~1u;
    const u64 magW = (0x100000000ull + W - 1) / W, magI = (0x100000000ull + WI - 1) / WI;
    const int T4 = T / 4;
    const int RF = T4 / (NWG * NT);
    const int RQ = (T4 - RF * NWG * NT + NWG - 1) / NWG;
    int dev = 0, cus = 0;
    (void)hipGetDevice(&dev);
    (void)hipDeviceGetAttribute(&cus, hipDeviceAttributeMultiprocessorCount, dev);
    const bool fused = De == 4 && NR == 8 && L == 1 && E % 2 == 0 && T % 4 == 0 && RF < NQ && W <= (u32)MAXW && WI <= (u32)MAXWI &&
                       magW < (1u << 24) && magI < (1u << 24) &&
                       (u64)E * (magW * W - 0x100000000ull) < 0x100000000ull && (u64)E * (magI * WI - 0x100000000ull) < 0x100000000ull &&
                       ws_size >= WS_END && cus >= NWG;
    if (fused) {
        const float rfac = 1.2f * (7.0f + 4.5f * sqrtf((float)T / ((float)E * NH))) / (float)E;
        auto kf = RF == NQ - 1 ? gemnet_fused_kernel<true> : gemnet_fused_kernel<false>;
        (void)hipFuncSetAttribute((const void*)kf, hipFuncAttributeMaxDynamicSharedMemorySize, LDS_BYTES);
        kf<<<NWG, NT, LDS_BYTES, stream>>>((const u32x4*)d_in[1], (const u32x4*)d_in[2], (const u32x2*)d_in[4],
            (const u16*)d_in[6], (const u32x4*)d_in[0], (char*)d_ws, (const u16*)d_in[3],
            (const u32*)d_in[5], (const u32*)d_in[7], (const u32*)d_in[8], (const u32*)d_in[9], (const u32*)d_in[10],
            (u32x2*)d_out, T4, RF, RQ, E, W, (u32)magW, WI, (u32)magI, rfac);
    } else {
        (void)hipMemsetAsync(d_ws, 0, (size_t)E * L * De * 4, stream);
        gemnet_scatter_kernel<<<(unsigned)(((long long)T * De + 255) / 256), 256, 0, stream>>>((const u16*)d_in[0], (const int*)d_in[1],
            (const int*)d_in[2], (const u16*)d_in[4], (const u16*)d_in[6], (float*)d_ws, T, De, L);
        gemnet_edge_kernel<<<(unsigned)(((long long)E * De + 255) / 256), 256, 0, stream>>>((const u16*)d_in[0], (const u16*)d_in[3],
            (const u16*)d_in[5], (const u16*)d_in[7], (const u16*)d_in[8], (const u16*)d_in[9], (const u16*)d_in[10],
            (const float*)d_ws, (u16*)d_out, E, De, NR, L);
    }
}
```

```cpp
#include <hip/hip_runtime.h>
#include <hip/hip_bf16.h>
#include <stdint.h>
#include <math.h>

typedef unsigned short u16;
typedef unsigned int u32;
typedef unsigned long long u64;
typedef u32 u32x4 __attribute__((ext_vector_type(4)));
typedef u32 u32x2 __attribute__((ext_vector_type(2)));
typedef float f32x4 __attribute__((ext_vector_type(4)));
typedef float f32x2 __attribute__((ext_vector_type(2)));
typedef __bf16 bf16x2 __attribute__((ext_vector_type(2)));
typedef __attribute__((address_space(1))) u32 gu32;

#pragma clang diagnostic ignored "-Wint-to-pointer-cast"
#define PIN(x) asm volatile("" : "+v"(x))
#define LP(T, a) ((__attribute__((address_space(3))) T*)(u32)(a))
#define SCHED() __builtin_amdgcn_sched_barrier(0)
#define RSRC(p, bytes) __builtin_amdgcn_make_buffer_rsrc((void*)(p), 0, (bytes), 0x00020000)
#define AUX_NT 2
#define AUX_SC1 16

constexpr int NT = 1024;
constexpr int NWG = 256;
constexpr int NS = 7;
constexpr int NH = 8;
constexpr int NBKT = NWG * NS;
constexpr int NQ = 13;
constexpr int WQ = 1568;
constexpr int MAXW = NH * WQ;
constexpr int MAXWI = 7168;
constexpr int NSUB = 2 * NWG;
constexpr int MAXN = 7 * 4096;
constexpr int UMAX = (MAXN + 2 * NBKT) / 3 + 1;
constexpr int UPC = 10760;
constexpr int LDS_BYTES = 163840;
constexpr int P1_DUMMY = NBKT * 4;
constexpr int P1_SCR = P1_DUMMY + 256;
constexpr int P1_STG = P1_SCR + 256;
constexpr int P1_UA = (LDS_BYTES - P1_STG) / 16;
constexpr int ND = (P1_UA + NT - 1) / NT;
static_assert(UMAX < UPC && 3 * UMAX < 65535 && P1_UA % 32 == 0 && ND == 10, "geometry");
constexpr int P2_ACC = LDS_BYTES - MAXW * 8;
constexpr int LCAP = 3 * NT;
constexpr int P2_HEAD = LDS_BYTES - NS * LCAP * 4;
constexpr int P2_SCR = MAXWI * 8;
constexpr u32 AB16 = 0x4000u;
constexpr u32 ABIAS = AB16 * 0x10001u;
static_assert(P2_SCR == NT * 56 && P2_SCR + 1024 <= P2_ACC && P2_ACC <= P2_HEAD && P2_ACC < 65536 && P2_ACC + 0x1fff8 >= LDS_BYTES, "layout");

constexpr size_t WS_REC = 0;
constexpr size_t WS_OFFS = WS_REC + (size_t)NSUB * UPC * 16;
constexpr size_t WS_OFFX = WS_OFFS + (size_t)NSUB * 256 * 32;
constexpr size_t WS_STAT = WS_OFFX + (size_t)NSUB * 256 * 32;
constexpr size_t WS_EPOCH = WS_STAT + 4096;
constexpr size_t WS_PHASE = WS_EPOCH + 4096;
constexpr size_t WS_FLAG = WS_PHASE + 8 * 1024;
constexpr size_t WS_PART = WS_FLAG + 32 * 16 * 128;
constexpr size_t WS_PART16 = WS_PART + (size_t)32 * 64 * WQ * 16;
constexpr size_t WS_END = WS_PART16 + (size_t)32 * 64 * WQ * 8;

__device__ __forceinline__ float bf2f(u16 v) { return __uint_as_float(((u32)v) << 16); }
__device__ __forceinline__ u32 pk_bf16(float a, float b) {
    f32x2 v = {a, b};
    bf16x2 r = __builtin_convertvector(v, bf16x2);
    return __builtin_bit_cast(u32, r);
}
__device__ __forceinline__ float rbf16(float v) { return __uint_as_float(pk_bf16(v, v) << 16); }
__device__ __forceinline__ f32x2 rbf16x2(f32x2 v) {
    const u32 t = pk_bf16(v.x, v.y);
    return f32x2{__uint_as_float(t << 16), __uint_as_float(t & 0xffff0000u)};
}
template <int CTRL, int RM>
__device__ __forceinline__ u32 dpp0(u32 v) { return __builtin_amdgcn_update_dpp(0u, v, CTRL, RM, 0xf, false); }
__device__ __forceinline__ u32 row_incl_sum(u32 v) {
    v += dpp0<0x111, 0xf>(v);
    v += dpp0<0x112, 0xf>(v);
    v += dpp0<0x114, 0xf>(v);
    v += dpp0<0x118, 0xf>(v);
    return v;
}
__device__ __forceinline__ u32 half_incl_sum(u32 v) {
    v = row_incl_sum(v);
    return v + dpp0<0x142, 0xa>(v);
}
__device__ __forceinline__ float wave_incl_sumf(float v) {
    v += __uint_as_float(dpp0<0x111, 0xf>(__float_as_uint(v)));
    v += __uint_as_float(dpp0<0x112, 0xf>(__float_as_uint(v)));
    v += __uint_as_float(dpp0<0x114, 0xf>(__float_as_uint(v)));
    v += __uint_as_float(dpp0<0x118, 0xf>(__float_as_uint(v)));
    v += __uint_as_float(dpp0<0x142, 0xa>(__float_as_uint(v)));
    v += __uint_as_float(dpp0<0x143, 0xc>(__float_as_uint(v)));
    return v;
}
__device__ __forceinline__ u32 wave_incl_max(u32 v) {
    v = max(v, dpp0<0x111, 0xf>(v));
    v = max(v, dpp0<0x112, 0xf>(v));
    v = max(v, dpp0<0x114, 0xf>(v));
    v = max(v, dpp0<0x118, 0xf>(v));
    v = max(v, dpp0<0x142, 0xa>(v));
    v = max(v, dpp0<0x143, 0xc>(v));
    return v;
}
__device__ __forceinline__ u32 mulhi24(u32 v, u32 s) { u32 r; asm("v_mul_hi_u32_u24 %0, %1, %2" : "=v"(r) : "s"(s), "v"(v)); return r; }
__device__ __forceinline__ u32 mad24(u32 a, int s, u32 c) { u32 r; asm("v_mad_i32_i24 %0, %1, %2, %3" : "=v"(r) : "v"(a), "s"(s), "v"(c)); return r; }
__device__ __forceinline__ u32 bsel(u32 m, u32 a, u32 b) { return __builtin_amdgcn_bitop3_b32(m, a, b, 0xca); }
__device__ __forceinline__ u32 andor(u32 a, u32 m, u32 b) { return __builtin_amdgcn_bitop3_b32(a, m, b, 0xea); }
__device__ __forceinline__ u32 or3(u32 a, u32 b, u32 c) { return __builtin_amdgcn_bitop3_b32(a, b, c, 0xfe); }
__device__ __forceinline__ u32 shl3_16(u32 v) { u32 r; asm("v_lshlrev_b16 %0, 3, %1" : "=v"(r) : "v"(v)); return r; }
__device__ __forceinline__ u32 shl2_16(u32 v) { u32 r; asm("v_lshlrev_b16 %0, 2, %1" : "=v"(r) : "v"(v)); return r; }
__device__ __forceinline__ u32 div3(u32 v) { return (u32)__umul24(v, 43691u) >> 17; }
__device__ __forceinline__ u32 lds_add_rtn(u32 addr, u32 v) { return __hip_atomic_fetch_add(LP(u32, addr), v, __ATOMIC_RELAXED, __HIP_MEMORY_SCOPE_WORKGROUP); }
__device__ __forceinline__ float half_of(u32 w, int k) { return __uint_as_float((k & 1) ? (w & 0xffff0000u) : (w << 16)); }

__device__ __forceinline__ float rev_large(float x) {
    const u32 b = __float_as_uint(x) & 0x7fffffffu;
    const u32 k = min((b >> 23) - 134u, 127u), j = k >> 5;
    const u32 M = ((b >> 16) & 0x7fu) | 0x80u;
    const u32 hi = j == 0 ? 0x28be60dbu : j == 1 ? 0x9391054au : j == 2 ? 0x7f09d5f4u : 0x7d4d3770u;
    const u32 lo = j == 0 ? 0x9391054au : j == 1 ? 0x7f09d5f4u : j == 2 ? 0x7d4d3770u : 0x36d8a566u;
    const u32 f = (u32)(((((u64)hi << 32) | lo) << (k & 31u)) >> 32);
    const float r = (float)(M * f) * 2.3283064365386963e-10f;
    return b >= 0x7f800000u ? __uint_as_float(0x7fc00000u) : r;
}

template <bool FULL>
__global__ void __launch_bounds__(NT, 4)
gemnet_fused_kernel(const u32x4* __restrict__ idx_in, const u32x4* __restrict__ idx_out, const u32x2* __restrict__ angle,
                    const u16* __restrict__ ang_freq, const u32x4* __restrict__ m16, char* ws,
                    const u16* __restrict__ dist, const u32* __restrict__ centers, const u32* __restrict__ w_rbf,
                    const u32* __restrict__ w_sbf, const u32* __restrict__ w_down, const u32* __restrict__ w_up,
                    u32x2* __restrict__ out, int T4, int RF, int RQ, int E, u32 W, u32 magW, u32 WI, u32 magI, float rfac)
{
    const int tid = threadIdx.x;
    const int lane = tid & 63;
    const int w = __builtin_amdgcn_readfirstlane(tid >> 6);
    const int c = blockIdx.x;
    const int m_n16 = E >> 1;
    u32 warm = 0;
    {
        u64 kpc; u32 klen;
        asm volatile("s_getpc_b64 %0\n.Lks%c2:\n\ts_mov_b32 %1, .Lke%c2-.Lks%c2" : "=s"(kpc), "=s"(klen) : "i"((int)FULL));
        if (w == 13) warm = __builtin_amdgcn_raw_buffer_load_b32(RSRC(kpc, klen), lane < 32 ? (u32)(lane * 32 + (c >> 3)) * 128u : 0xfffffff0u, 0, 0);
        if (w == 12) {
            const u32 off = lane < 16 ? (u32)(WS_FLAG - WS_STAT) + ((c >> 3) * 16 + lane) * 128
                          : lane < 24 ? (u32)(WS_PHASE - WS_STAT) + (c & 7) * 1024 + (lane - 16) * 128 : (c >> 3) * 128;
            warm = __builtin_amdgcn_raw_buffer_load_b32(RSRC(ws + WS_STAT, (int)(WS_PART - WS_STAT)), off, 0, AUX_SC1);
        }
    }
    const __amdgpu_buffer_rsrc_t rI = RSRC(idx_in, T4 * 16), rO = RSRC(idx_out, T4 * 16), rA = RSRC(angle, T4 * 8);
    const int rem0 = RF * NWG * NT + c * RQ;
    const bool valid0 = tid < min(RQ, T4 - rem0);
    auto skip = [&](int i) -> bool { return i == 0 ? w * 64 >= RQ : (!FULL && i - 1 >= RF); };
    const int mper = (m_n16 + NWG - 1) / NWG;
    const int m0i = min(c * mper, m_n16);
    const __amdgpu_buffer_rsrc_t rT = RSRC(ws + WS_OFFS, (int)(WS_FLAG - WS_OFFS));

    u32x4 rin[NQ], rout[NQ];
    u32x2 rang[NQ];
    u32 P[NQ][4], B8[NQ], BK[NQ][2];

    const u32 t16 = tid * 16, t8 = tid * 8;
    const u32 epoch = __builtin_amdgcn_raw_buffer_load_b32(rT, (int)(WS_EPOCH - WS_OFFS), 0, AUX_SC1);
    const u32x4 mv = __builtin_amdgcn_raw_buffer_load_b128(RSRC(m16 + m0i, min(mper, m_n16 - m0i) * 16), t16, 0, 0);
    auto issue = [&](int i) {
        if (i == 0) {
            const u32 q = valid0 ? rem0 + tid : 0x0fffffffu;
            rin[i] = __builtin_amdgcn_raw_buffer_load_b128(rI, q * 16, 0, AUX_NT);
            rout[i] = __builtin_amdgcn_raw_buffer_load_b128(rO, q * 16, 0, AUX_NT);
            rang[i] = __builtin_amdgcn_raw_buffer_load_b64(rA, q * 8, 0, AUX_NT);
        } else if (!skip(i)) {
            const u32 qb = ((i - 1) * NWG + c) * NT;
            rin[i] = __builtin_amdgcn_raw_buffer_load_b128(rI, t16, qb * 16, AUX_NT);
            rout[i] = __builtin_amdgcn_raw_buffer_load_b128(rO, t16, qb * 16, AUX_NT);
            rang[i] = __builtin_amdgcn_raw_buffer_load_b64(rA, t8, qb * 8, AUX_NT);
        }
    };
    issue(0); issue(1);
    const float freq = bf2f(ang_freq[0]);
    SCHED();
    const u32x4 z4 = {0, 0, 0, 0};
#pragma unroll
    for (int k = 0; k < LDS_BYTES / 16 / NT; k++) *LP(u32x4, t16 + k * NT * 16) = z4;
    __syncthreads();
    asm volatile("" :: "v"(warm));
    const u32 tag = __builtin_amdgcn_readfirstlane(epoch) + 0x9e3779b9u;
    {
        u32 lo[4], hi[4], mx = 0;
#pragma unroll
        for (int e = 0; e < 4; e++) { lo[e] = (mv[e] << 16) & 0x7fffffffu; hi[e] = mv[e] & 0x7fff0000u; mx = max(mx, max(lo[e], hi[e])); }
        mx = wave_incl_max(mx);
        f32x4 sa = {__uint_as_float(lo[0]) + __uint_as_float(lo[2]), __uint_as_float(hi[0]) + __uint_as_float(hi[2]),
                    __uint_as_float(lo[1]) + __uint_as_float(lo[3]), __uint_as_float(hi[1]) + __uint_as_float(hi[3])};
#pragma unroll
        for (int e = 0; e < 4; e++) sa[e] = wave_incl_sumf(sa[e]);
        if (lane == 63) {
            __hip_atomic_fetch_max(LP(u32, P1_SCR + 64), mx, __ATOMIC_RELAXED, __HIP_MEMORY_SCOPE_WORKGROUP);
            *LP(float, P1_SCR + 128 + w * 4) = fmaxf(fmaxf(sa[0], sa[1]), fmaxf(sa[2], sa[3]));
        }
    }
    const bool unit_freq = __builtin_amdgcn_readfirstlane(__float_as_uint(freq)) == 0x3f800000u;
    u32 nanacc = 0;
    u32 vm8 = 0x80008000u, vmt = 0xf8000000u, v12 = 12;
    PIN(vm8); PIN(vmt); PIN(v12);

    auto encode = [&](int i) {
        if (skip(i)) return;
        u32 x01 = rang[i].x, x23 = rang[i].y;
        if (!unit_freq) {
            x01 = pk_bf16(half_of(x01, 0) * freq, half_of(x01, 1) * freq);
            x23 = pk_bf16(half_of(x23, 0) * freq, half_of(x23, 1) * freq);
        }
        const float x[4] = {half_of(x01, 0), half_of(x01, 1), half_of(x23, 0), half_of(x23, 1)};
        float r[4];
#pragma unroll
        for (int k = 0; k < 4; k++) r[k] = x[k] * 0.15915494309189535f;
        const float rm = fmaxf(fmaxf(fabsf(r[0]), fabsf(r[1])), fmaxf(fabsf(r[2]), fabsf(r[3])));
        if (__builtin_expect(__any(rm >= 20.0f), 0)) {
#pragma unroll
            for (int k = 0; k < 4; k++) r[k] = fabsf(x[k]) >= 128.0f ? rev_large(x[k]) : r[k];
        }
        const u32 c01 = pk_bf16(__builtin_amdgcn_cosf(r[0]), __builtin_amdgcn_cosf(r[1]));
        const u32 c23 = pk_bf16(__builtin_amdgcn_cosf(r[2]), __builtin_amdgcn_cosf(r[3]));
        nanacc = or3(c01, c23, nanacc);
        PIN(nanacc);
        B8[i] = __builtin_amdgcn_perm(c23, c01, 0x06040200u);
        const u32 t01 = bsel(vm8, c01, c01 << 3), t23 = bsel(vm8, c23, c23 << 3);
        const u32 F[4] = {t01 << 16, t01, t23 << 16, t23};
        u32 bk[4];
#pragma unroll
        for (int k = 0; k < 4; k++) {
            const u32 vi = rin[i][k], vo = rout[i][k];
            const u32 q = mulhi24(vi, magI);
            const u32 p = mulhi24(vo, magW);
            const u32 li = mad24(q, -(int)WI, vi);
            const u32 lo = mad24(p, -(int)W, vo);
            P[i][k] = bsel(vmt, F[k], ((lo << 13) + li) ^ 0x07ffe000u);
            bk[k] = shl2_16((q << 5) + p);
            if (i == 0) bk[k] = valid0 ? bk[k] : (u32)(P1_DUMMY + lane * 4);
            lds_add_rtn(bk[k], i < 7 ? 1u : 0x10000u);
        }
        BK[i][0] = bk[0] | (bk[1] << 16);
        BK[i][1] = bk[2] | (bk[3] << 16);
        PIN(BK[i][0]); PIN(BK[i][1]); PIN(B8[i]); PIN(P[i][0]); PIN(P[i][1]); PIN(P[i][2]); PIN(P[i][3]);
    };
    const __amdgpu_buffer_rsrc_t rR = RSRC(ws + WS_REC, NSUB * UPC * 16);
    auto scan = [&](int sub) -> u32 {
        u32 un[7], st[7];
        if (w < 4) {
#pragma unroll
            for (int k = 0; k < 7; k++) un[k] = div3((*LP(u32, tid * 4 + k * 1024) >> (16 * sub)) + 2);
            u32 run = 0;
#pragma unroll
            for (int j = 0; j < 4; j++) {
                const u32 v = half_incl_sum(un[2 * j] | (j < 3 ? un[2 * j + 1] << 16 : 0u));
                const u32 t0 = __builtin_amdgcn_readlane(v, 31), t1 = __builtin_amdgcn_readlane(v, 63);
                const u32 tt = lane < 32 ? t0 : t1;
                st[2 * j] = run + (v & 0xffffu) - un[2 * j];
                run += tt & 0xffffu;
                if (j < 3) { st[2 * j + 1] = run + (v >> 16) - un[2 * j + 1]; run += tt >> 16; }
            }
            if ((lane & 31) == 0) *LP(u32, P1_SCR + (tid >> 5) * 4) = run;
        }
        if (w == 8) *LP(u32, P1_DUMMY + lane * 4) = 0xffffu;
        __syncthreads();
        const u32 ht = *LP(u32, P1_SCR + (lane & 7) * 4);
        u32 hs = ht + dpp0<0x111, 0xf>(ht);
        hs += dpp0<0x112, 0xf>(hs); hs += dpp0<0x114, 0xf>(hs);
        const u32 U = __builtin_amdgcn_readlane(hs, 7);
        if (w < 4) {
            const u32 b0 = __builtin_amdgcn_readlane(hs - ht, 2 * w), b1 = __builtin_amdgcn_readlane(hs - ht, 2 * w + 1);
            const u32 base = lane < 32 ? b0 : b1;
            u32 nn[7], dd[7], sat = *LP(u32, P1_SCR + 96);
#pragma unroll
            for (int k = 0; k < 7; k++) {
                st[k] += base;
                *LP(u32, tid * 4 + k * 1024) = st[k] * 3;
                nn[k] = min(un[k], 31u);
                dd[k] = k ? min(st[k] - st[k - 1], 1023u) : 0u;
                sat |= (nn[k] == 31u) | (k != 4 && dd[k] == 1023u);
            }
            if (sat) nn[0] = 31u;
            const u32 x0 = st[0] | (dd[1] << 14) | (nn[0] << 24) | (nn[3] << 29), z0 = dd[2] | (dd[3] << 10) | (nn[1] << 20) | (nn[2] << 25) | ((nn[3] >> 3) << 30);
            const u32 x1 = st[4] | (dd[5] << 14) | (nn[4] << 24), z1 = dd[6] | (nn[5] << 20) | (nn[6] << 25);
#pragma unroll
            for (int j = 0; j < 2; j++) {
                const int src = (j * 32 + (lane >> 1)) * 4;
                const u32 a0 = __builtin_amdgcn_ds_bpermute(src, x0), a1 = __builtin_amdgcn_ds_bpermute(src, x1);
                const u32 c0 = __builtin_amdgcn_ds_bpermute(src, z0), c1 = __builtin_amdgcn_ds_bpermute(src, z1);
                const u32x4 o = {(lane & 1) ? a1 : a0, tag, (lane & 1) ? c1 : c0, tag};
                __builtin_amdgcn_raw_buffer_store_b128(o, rT, (sub * NWG + c) * 8192 + w * 2048 + j * 1024 + lane * 16, 0, AUX_SC1);
            }
            if (sat) {
#pragma unroll
                for (int k = 0; k < 7; k++)
                    __builtin_amdgcn_raw_buffer_store_b32(st[k] | (un[k] << 16), rT, (int)(WS_OFFX - WS_OFFS) + (sub * NWG + c) * 8192 + tid * 32 + k * 4, 0, AUX_SC1);
            }
        }
        if (U > (u32)P1_UA) {
            for (u32 u = P1_UA + tid; u < U; u += NT) __builtin_amdgcn_raw_buffer_store_b128(z4, rR, ((sub * NWG + c) * UPC + u) * 16, 0, AUX_SC1);
            asm volatile("s_waitcnt vmcnt(0)" ::: "memory");
        }
        __syncthreads();
        return U;
    };
    u32 pos[NQ][4];
    auto cursor = [&](int i) {
        if (skip(i)) return;
        PIN(BK[i][0]); PIN(BK[i][1]);
        const u32 inc = i > 0 || valid0 ? 1u : 0u;
        pos[i][0] = lds_add_rtn(BK[i][0] & 0xffffu, inc); pos[i][1] = lds_add_rtn(BK[i][0] >> 16, inc);
        pos[i][2] = lds_add_rtn(BK[i][1] & 0xffffu, inc); pos[i][3] = lds_add_rtn(BK[i][1] >> 16, inc);
    };
    auto scatter = [&](int i, bool big) {
        if (skip(i)) return;
#pragma unroll
        for (int k = 0; k < 4; k++) {
            const u32 ps = i < 7 ? (pos[i][k] & 0xffffu) : pos[i][k];
            const u32 t = div3(ps), zz = t >> 1;
            *LP(u32, __builtin_amdgcn_bitop3_b32((ps + t) << 2, zz, v12, 0x78) + P1_STG) = P[i][k];
            const u32 src = (k & 1) ? (B8[i] >> 8) : B8[i];
            *LP(unsigned char, __builtin_amdgcn_bitop3_b32(mad24(t, 13, ps), zz, v12, 0xf2) + P1_STG) = (unsigned char)((k & 2) ? (src >> 16) : src);
        }
        if (big) {
            const u32 g = ((i >= 7) * NWG + c) * UPC * 16;
#pragma unroll
            for (int k = 0; k < 4; k++) {
                const u32 ps = pos[i][k] & 0xffffu, t = div3(ps);
                if (t >= (u32)P1_UA && ps != 0xffffu) {
                    __builtin_amdgcn_raw_buffer_store_b32(P[i][k], rR, g + (ps + t) * 4, 0, AUX_SC1);
                    __builtin_amdgcn_raw_buffer_store_b8((unsigned char)(B8[i] >> (8 * k)), rR, g + 13 * t + ps + 12, 0, AUX_SC1);
                }
            }
        }
    };
    const u32 sw1 = (tid & 8) ? ~0u : 0u, sw2 = (tid & 16) ? ~0u : 0u;
    auto unswz = [&](u32x4 v) -> u32x4 {
        const u32x4 a = {bsel(sw1, v.y, v.x), bsel(sw1, v.x, v.y), bsel(sw1, v.w, v.z), bsel(sw1, v.z, v.w)};
        return u32x4{bsel(sw2, a.z, a.x), bsel(sw2, a.w, a.y), bsel(sw2, a.x, a.z), bsel(sw2, a.y, a.w)};
    };

    auto piece = [&](int k) -> u32 {
        u32 r;
        asm volatile("v_add_u32 %0, %1, %2" : "=v"(r) : "i"(k * NT * 16), "v"(t16));
        return r;
    };

#pragma unroll
    for (int i = 0; i < 7; i++) {
        issue(i + 2);
        SCHED();
        encode(i);
        SCHED();
    }
    __syncthreads();
    const u32 UA_ = scan(0);
    if (tid == 0) {
        float t = 0.f;
#pragma unroll
        for (int k = 0; k < 4; k++) { const f32x4 v = *LP(f32x4, P1_SCR + 128 + k * 16); t += (v.x + v.y) + (v.z + v.w); }
        __builtin_amdgcn_raw_buffer_store_b128(u32x4{*LP(u32, P1_SCR + 64), tag, __float_as_uint(t), tag}, rT, (int)(WS_STAT - WS_OFFS) + c * 16, 0, AUX_SC1);
    }
    {
        const bool big = UA_ > (u32)P1_UA;
        cursor(0);
#pragma unroll
        for (int i = 0; i < 7; i++) {
            if (i + 1 < 7) cursor(i + 1);
            SCHED();
            scatter(i, big);
            SCHED();
            if (i & 1) encode(7 + i / 2);
            else issue(9 + i / 2);
            SCHED();
        }
    }
    __syncthreads();
    u32x4 DR[ND];
#pragma unroll
    for (int k = 0; k < ND; k++) {
        DR[k] = *LP(u32x4, P1_STG + t16 + k * NT * 16);
        *LP(u32x4, P1_STG + t16 + k * NT * 16) = z4;
    }
    SCHED();
    const __amdgpu_buffer_rsrc_t rS0 = RSRC(ws + WS_REC + (size_t)(u32)(c * UPC) * 16u, min((UA_ + 7) & ~7u, (u32)P1_UA) * 16);
    const int h = c & 7, p = c >> 3;
    const int task = h * 32 + p;
    const bool sg = lane >= 32;
    const int cc = w * 32 + (lane & 31);
    u32x4 o4 = {0, 0, 0, 0}, mxv = {0, tag, 0, tag};
    auto load_tables = [&]() {
        o4 = __builtin_amdgcn_raw_buffer_load_b128(rT, cc * 8192 + task * 32 + (sg ? 16 : 0), 0, AUX_SC1);
        if (w < 4) mxv = __builtin_amdgcn_raw_buffer_load_b128(rT, (int)(WS_STAT - WS_OFFS) + t16, 0, AUX_SC1);
        SCHED();
    };
    encode(10); SCHED();
    encode(11); SCHED();
    encode(12); SCHED();
    if (nanacc & 0x40004000u) *LP(u32, P1_SCR + 96) = 1;
    __syncthreads();
    const u32 UB_ = scan(1);
    if (FULL && w < 8) load_tables();
    {
        const bool big = UB_ > (u32)P1_UA;
        cursor(7);
#pragma unroll
        for (int i = 7; i < NQ; i++) {
            if (i + 1 < NQ) cursor(i + 1);
            SCHED();
#pragma unroll
            for (int k = 2 * (i - 8); k < 2 * (i - 7); k++)
                if (k >= 0) { __builtin_amdgcn_raw_buffer_store_b128(unswz(DR[k]), rS0, piece(k), 0, AUX_SC1); SCHED(); }
            scatter(i, big);
            SCHED();
        }
    }
    __syncthreads();

#pragma unroll
    for (int k = 0; k < ND; k++) DR[k] = *LP(u32x4, P1_STG + t16 + k * NT * 16);
    const __amdgpu_buffer_rsrc_t rS1 = RSRC(ws + WS_REC + (size_t)(u32)((NWG + c) * UPC) * 16u, min((UB_ + 7) & ~7u, (u32)P1_UA) * 16);
    auto stores = [&](int k0, int k1) {
#pragma unroll
        for (int k = k0; k < k1; k++) { __builtin_amdgcn_raw_buffer_store_b128(unswz(DR[k]), rS1, piece(k), 0, AUX_SC1); SCHED(); }
    };
    auto arrive = [&](int ad) { asm volatile("" ::: "memory"); SCHED(); if (lane == 0) lds_add_rtn(ad, 1u); SCHED(); };
    auto await = [&](int ad) {
        SCHED();
        while (__builtin_amdgcn_readfirstlane(__hip_atomic_load(LP(u32, ad), __ATOMIC_RELAXED, __HIP_MEMORY_SCOPE_WORKGROUP)) < 16u)
            __builtin_amdgcn_s_sleep(2);
        asm volatile("" ::: "memory");
        SCHED();
    };
    if (!FULL || w >= 8) load_tables();
    stores(0, 1);
    __syncthreads();
#pragma unroll
    for (int k = 0; k < 5; k++) *LP(u32x4, P2_HEAD + t16 + k * NT * 16) = z4;
    if (w < 4) *LP(u32x4, P2_HEAD + t16 + 5 * NT * 16) = z4;
    if (tid == 0) *LP(u32, P2_SCR + 400) = 0;
    if (tid < 16) *LP(u32, P2_SCR + 448 + tid * 4) = 0;
    SCHED();
#pragma unroll 1
    for (u32 spins = 0; spins < (1u << 20) && !__all(o4.y == tag && o4.w == tag && mxv.y == tag && mxv.w == tag); spins++) {
        asm volatile("" ::: "memory");
        load_tables();
    }
    u32 n[4], inc[4], st[4];
    n[0] = (o4.x >> 24) & 31u; n[1] = (o4.z >> 20) & 31u; n[2] = (o4.z >> 25) & 31u; n[3] = (o4.x >> 29) | ((o4.z >> 30) << 3);
    st[0] = o4.x & 0x3fffu; st[1] = st[0] + ((o4.x >> 14) & 1023u); st[2] = st[1] + (o4.z & 1023u); st[3] = st[2] + ((o4.z >> 10) & 1023u);
#pragma unroll
    for (int k = 0; k < 4; k++) inc[k] = half_incl_sum(k == 0 && n[0] == 31u ? (u32)LCAP + 1 : n[k]);
    if ((lane & 31) == 31) {
#pragma unroll
        for (int k = 0; k < 4; k++) *LP(u32, P2_SCR + 512 + ((sg * 4 + k) * 16 + w) * 4) = inc[k];
    }
    if (w < 4) {
        const u32 mx = wave_incl_max(mxv.x);
        const float sm = wave_incl_sumf(__uint_as_float(mxv.z));
        if (lane == 63) { *LP(u32, P2_SCR + 384 + w * 4) = mx; *LP(float, P2_SCR + 416 + w * 4) = sm; }
    }
    arrive(P1_SCR + 72);
    stores(1, 4);
    await(P1_SCR + 72);
    u64 m3;
    bool slow;
    {
        const u32 ta = *LP(u32, P2_SCR + 512 + lane * 4), tb = *LP(u32, P2_SCR + 768 + lane * 4);
        u32 ia = row_incl_sum(ta), ib = row_incl_sum(tb);
        slow = __any(max(ia, ib) > (u32)LCAP);
        m3 = (__ballot(ia > (u32)(2 * NT + w * 64)) & 0x8000800080008000ull) | ((__ballot(ib > (u32)(2 * NT + w * 64)) & 0x8000800080008000ull) >> 1);
        const u32 tt = __builtin_amdgcn_readlane(w < 4 ? ia : ib, (w & 3) * 16 + 15);
        ia -= ta; ib -= tb;
        bool more = false;
        u32 nx[4], en[4], dl[4];
        const u32 hb0 = P2_HEAD + (sg ? 4 * LCAP * 4 : 0);
#pragma unroll
        for (int k = 0; k < 4; k++) {
            const u32 ca = __builtin_amdgcn_readlane(ia, k * 16 + w), cb = __builtin_amdgcn_readlane(ib, k * 16 + w);
            const u32 carry = sg ? cb : ca;
            const u32 ex = carry + inc[k] - n[k];
            en[k] = min(carry + inc[k], (u32)LCAP);
            dl[k] = ((u32)cc * UPC + st[k] - ex) * 16;
            const u32 hb = hb0 + k * LCAP * 4;
            *LP(u32, ex < en[k] ? hb + ex * 4 : LDS_BYTES) = dl[k];
            const u32 b1 = (ex | 7u) + 1;
            *LP(u32, b1 < en[k] ? hb + b1 * 4 : LDS_BYTES) = dl[k];
            nx[k] = b1 + 8;
            more |= nx[k] < en[k];
        }
        if (more) {
#pragma unroll
            for (int k = 0; k < 4; k++)
                for (u32 ps = nx[k]; ps < en[k]; ps += 8) *LP(u32, hb0 + k * LCAP * 4 + ps * 4) = dl[k];
        }
        if (w < NS) {
            for (u32 ps = (tt & ~7u) + lane * 8; ps < (u32)LCAP; ps += 512) *LP(u32, P2_HEAD + (w * LCAP + max(ps, tt)) * 4) = 0xf0000000u;
        }
    }
    const u32x4 mq = *LP(u32x4, P2_SCR + 384);
    const u32 maxbits = __builtin_amdgcn_readfirstlane(max(max(mq[0], mq[1]), max(mq[2], mq[3])));
    const f32x4 sq = *LP(f32x4, P2_SCR + 416);
    const float rtyp = rfac * ((sq.x + sq.y) + (sq.z + sq.w));
    const float range = fmaxf(rtyp, 2.002f * __uint_as_float(maxbits));
    slow = slow || maxbits < 0x10000000u || maxbits > 0x70000000u || __any(range > 2.0f * rtyp);
    arrive(P1_SCR + 76);
    stores(4, 7);
    await(P1_SCR + 76);
    u32 hv[NS][3];
#pragma unroll
    for (int s = 0; s < NS; s++)
#pragma unroll
        for (int t = 0; t < 3; t++) hv[s][t] = *LP(u32, P2_HEAD + (s * LCAP + t * NT + tid) * 4);
    stores(7, ND);
    asm volatile("s_waitcnt vmcnt(0) lgkmcnt(0)\n\ts_barrier" ::: "memory");
    if (w == 14 && lane < 8) __builtin_amdgcn_raw_buffer_store_b32(tag, rT, (int)(WS_PHASE - WS_OFFS) + lane * 1024 + c * 4, 0, AUX_SC1);
    const __amdgpu_buffer_rsrc_t rM = RSRC(m16, m_n16 * 16);
    u32x4 wr[NS][3]; u32x2 wr3[NS];
    auto wload = [&](int j) {
        const int s = j >> 2, k = j & 3;
        const u32 base = (u32)(s * NH + h) * WI * 8;
        if (k < 3) wr[s][k] = __builtin_amdgcn_raw_buffer_load_b128(rM, base + k * 16384 + t16, 0, 0);
        else wr3[s] = __builtin_amdgcn_raw_buffer_load_b64(rM, base + 49152 + t8, 0, 0);
    };
    auto wstore = [&](int s) {
#pragma unroll
        for (int k = 0; k < 3; k++) *LP(u32x4, k * 16384 + t16) = wr[s][k];
        *LP(u32x2, 49152 + t8) = wr3[s];
    };
#pragma unroll
    for (int j = 0; j < 4; j++) wload(j);
    SCHED();
    {
        const u32x4 b4 = {ABIAS, ABIAS, ABIAS, ABIAS};
#pragma unroll
        for (int k = 0; k < 6; k++) *LP(u32x4, P2_ACC + t16 + k * NT * 16) = b4;
        if (w < 2) *LP(u32x4, P2_ACC + t16 + 6 * NT * 16) = b4;
    }
    u32 p16[3] = {t16, t16 + NT * 16, t16 + 2 * NT * 16};
    PIN(p16[1]); PIN(p16[2]);
    auto expand = [&](int s) {
#pragma unroll
        for (int t = 0; t < 3; t++) {
            u32 v = hv[s][t];
            v = max(v, dpp0<0x111, 0xf>(v));
            v = max(v, dpp0<0x112, 0xf>(v));
            v = max(v, dpp0<0x114, 0xf>(v));
            hv[s][t] = v + p16[t];
        }
#pragma unroll
        for (int t = 0; t < 3; t++) PIN(hv[s][t]);
    };
    expand(0); expand(1);
    SCHED();
    wstore(0);
    asm volatile("" :: "s"(centers[0]), "s"(w_sbf[0]), "s"(w_rbf[0]), "s"(w_down[0]), "s"(w_up[0]));

    const float quantum = __uint_as_float(__builtin_amdgcn_readfirstlane(__float_as_uint(range * (1.0f / AB16))));
    u32 ovf = 0;
    auto handoff = [&]() {
        if (w == 15) {
#pragma unroll 1
            for (u32 spins = 0; spins < (1u << 20); spins++) {
                asm volatile("" ::: "memory");
                const u32x4 v = __builtin_amdgcn_raw_buffer_load_b128(rT, (int)(WS_PHASE - WS_OFFS) + h * 1024 + lane * 16, 0, AUX_SC1);
                if (__all(v.x == tag && v.y == tag && v.z == tag && v.w == tag)) break;
                __builtin_amdgcn_s_sleep(4);
            }
        }
        asm volatile("s_waitcnt lgkmcnt(0)\n\ts_barrier" ::: "memory");
        if (c == 0 && tid == 0) __builtin_amdgcn_raw_buffer_store_b32(tag, rT, (int)(WS_EPOCH - WS_OFFS), 0, AUX_SC1);
    };
    if (!slow) {
        float S = (float)AB16 / range;
        float MAG = 8421376.0f;
        u32 vmf = 0x8f000000u, v30 = 0x30000000u, vma = 0x1fff8u;
        PIN(MAG); PIN(vmf); PIN(v30); PIN(S); PIN(vma);
        auto third = [&](int s) -> bool { return (m3 >> (16 * (s & 3) + 15 - (s >> 2))) & 1; };
        u32x4 RP[NS][3];
        u32x2 G[NS][9];
        u64 OLD[NS][9];
        constexpr int wsch[NS + 1] = {16, 18, 20, 23, 25, 28, 28, 28};
#pragma unroll
        for (int j = 4; j < wsch[0]; j++) wload(j);
        SCHED();
        handoff();
        auto rload = [&](int s, int t) {
            if (s < NS && (t < 2 || third(s))) RP[s][t] = __builtin_amdgcn_raw_buffer_load_b128(rR, hv[s][t], 0, AUX_SC1);
            SCHED();
        };
        auto gather = [&](int s, int t) {
#pragma unroll
            for (int k = 0; k < 3; k++) G[s][3 * t + k] = *LP(u32x2, shl3_16(RP[s][t][k]));
        };
        auto accum = [&](int s, int r) {
            const int t = r / 3, k = r % 3;
            const u32 Pk = RP[s][t][k];
            const u32 lowb = __builtin_amdgcn_perm(v30, RP[s][t][3], 0x07000c0cu | ((u32)k << 16));
            const float cs = __uint_as_float(andor((u32)((int)Pk >> 3), vmf, lowb)) * S;
            const u32 x0 = __float_as_uint(__builtin_fmaf(half_of(G[s][r].x, 0), cs, MAG));
            const u32 x1 = __float_as_uint(__builtin_fmaf(half_of(G[s][r].x, 1), cs, MAG));
            const u32 x2 = __float_as_uint(__builtin_fmaf(half_of(G[s][r].y, 0), cs, MAG));
            const u32 x3 = __float_as_uint(__builtin_fmaf(half_of(G[s][r].y, 1), cs, MAG));
            const u32x2 pa = {__builtin_amdgcn_perm(x1, x0, 0x05040100u), __builtin_amdgcn_perm(x3, x2, 0x05040100u)};
            const u64 a = __builtin_bit_cast(u64, pa) - 0x8000800080008000ull;
            const u32 ad = __builtin_amdgcn_bitop3_b32(Pk >> 10, vma, vma, 0x08);
            OLD[s][r] = __hip_atomic_fetch_add(LP(u64, ad + P2_ACC), a, __ATOMIC_RELAXED, __HIP_MEMORY_SCOPE_WORKGROUP);
            SCHED();
        };
        auto check = [&](int s, int r) {
            const u32x2 y = __builtin_bit_cast(u32x2, OLD[s][r]);
            ovf = or3(y.x, y.y, ovf);
            PIN(ovf);
        };
        rload(0, 0); rload(0, 1); rload(0, 2);
#pragma unroll
        for (int s = 2; s < NS; s++) expand(s);
#pragma unroll
        for (int s = 0; s < NS; s++) {
            SCHED();
            if (s == 0) {
                gather(0, 0); SCHED();
                rload(1, 2); rload(1, 0);
                accum(0, 0); accum(0, 1); accum(0, 2);
                gather(0, 1); SCHED();
                rload(1, 1);
                if (third(0)) gather(0, 2);
                arrive(P2_SCR + 448);
            } else {
                await(P2_SCR + 448 + (2 * s - 1) * 4);
                if (third(s)) gather(s, 2);
                gather(s, 0); gather(s, 1);
                if (s + 1 < NS) arrive(P2_SCR + 448 + 2 * s * 4);
                rload(s + 1, 1);
                check(s - 1, 0); check(s - 1, 1);
                accum(s - 1, 3); accum(s - 1, 4); accum(s - 1, 5);
                check(s - 1, 2);
            }
            if (s + 1 < NS) {
                await(P2_SCR + 448 + 2 * s * 4);
                wstore(s + 1);
                arrive(P2_SCR + 448 + (2 * s + 1) * 4);
            }
            const int wn = wsch[s + 1] - wsch[s];
            if (wn > 0) { wload(wsch[s]); SCHED(); }
            if (s > 0) { check(s - 1, 3); check(s - 1, 4); check(s - 1, 5); }
            if (third(s)) {
                accum(s, 6); accum(s, 7); accum(s, 8);
                check(s, 6); check(s, 7); check(s, 8);
                SCHED();
            }
            rload(s + 2, 2);
            if (wn > 2) { wload(wsch[s] + 1); SCHED(); }
            if (s > 0) accum(s, 0);
            if (wn > 0) { wload(wsch[s + 1] - 1); SCHED(); }
            rload(s + 2, 0);
            if (s > 0) { accum(s, 1); accum(s, 2); }
        }
        accum(NS - 1, 3); accum(NS - 1, 4); accum(NS - 1, 5);
#pragma unroll
        for (int r = 0; r < 6; r++) check(NS - 1, r);
    } else {
        handoff();
    }
    const u32 e0 = (u32)p * W + (u32)h * WQ;
    const u32 ne = min(min((u32)WQ, W - min(W, (u32)h * WQ)), (u32)E - min((u32)E, e0));
    const bool act = w * 128 < WQ;
    u32x4 em = {0, 0, 0, 0}; u32 ed = 0;
    if (act) {
        em = __builtin_amdgcn_raw_buffer_load_b128(rM, (e0 + tid * 2) * 8, 0, 0);
        ed = __builtin_amdgcn_raw_buffer_load_b32(RSRC(dist, E * 2), (e0 + tid * 2) * 2, 0, 0);
    }
    if (ovf & 0x80008000u) *LP(u32, P2_SCR + 400) = 1;
    __syncthreads();
    slow = slow || __builtin_amdgcn_readfirstlane(*LP(u32, P2_SCR + 400)) != 0;

    u32 cw[4], wsb[16], wrb[16], wdn[8], wup[8];
#pragma unroll
    for (int k = 0; k < 4; k++) cw[k] = centers[k];
#pragma unroll
    for (int k = 0; k < 16; k++) { wsb[k] = w_sbf[k]; wrb[k] = w_rbf[k]; }
#pragma unroll
    for (int k = 0; k < 8; k++) { wdn[k] = w_down[k]; wup[k] = w_up[k]; }
    const __amdgpu_buffer_rsrc_t rX = RSRC(ws + WS_PART + (size_t)p * 64 * WQ * 16, 64 * WQ * 16);
    const __amdgpu_buffer_rsrc_t rY = RSRC(ws + WS_PART16 + (size_t)p * 64 * WQ * 8, 64 * WQ * 8);
    u32x4 own = {ABIAS, ABIAS, ABIAS, ABIAS};
    f32x4 sm0 = {0, 0, 0, 0}, sm1 = {0, 0, 0, 0};
    if (!slow) {
        for (int k = 0; k < w; k++) __builtin_amdgcn_s_sleep(4);
        if (tid < WQ / 2) {
#pragma unroll
            for (int q = 1; q < NH; q++) {
                const u32 hd = (h + q) & 7;
                const u32x4 v = *LP(u32x4, P2_ACC + (hd * WQ + tid * 2) * 8);
                __builtin_amdgcn_raw_buffer_store_b128(v, rY, ((hd * 8 + h) * WQ + tid * 2) * 8, 0, AUX_SC1);
            }
            own = *LP(u32x4, P2_ACC + (h * WQ + tid * 2) * 8);
        }
    } else {
        const u16* m2 = (const u16*)m16;
        for (int cmp = 0; cmp < 4; cmp++) {
            __syncthreads();
            for (u32 k = tid; k < MAXW / 2; k += NT) *LP(u32x4, P2_ACC + k * 16) = z4;
            __syncthreads();
            for (int r = w; r < NSUB * NS; r += 16) {
                const int ck = r / NS, s = r - ck * NS;
                const u32 e0x = __builtin_amdgcn_raw_buffer_load_b32(rT, ck * 8192 + task * 32, 0, AUX_SC1);
                const u32x4 oe = __builtin_amdgcn_raw_buffer_load_b128(rT, ck * 8192 + task * 32 + (s >> 2) * 16, 0, AUX_SC1);
                const int k4 = s & 3;
                u32 b0 = (oe.x & 0x3fffu) + (k4 > 0 ? (oe.x >> 14) & 1023u : 0) + (k4 > 1 ? oe.z & 1023u : 0) + (k4 > 2 ? (oe.z >> 10) & 1023u : 0);
                u32 b1 = k4 == 0 ? (oe.x >> 24) & 31u : k4 == 1 ? (oe.z >> 20) & 31u : k4 == 2 ? (oe.z >> 25) & 31u : (oe.x >> 29) | ((oe.z >> 30) << 3);
                if (((e0x >> 24) & 31u) == 31u) {
                    const u32 x = __builtin_amdgcn_raw_buffer_load_b32(rT, (int)(WS_OFFX - WS_OFFS) + ck * 8192 + task * 32 + s * 4, 0, AUX_SC1);
                    b0 = x & 0xffffu; b1 = x >> 16;
                }
                b0 += (u32)ck * UPC; b1 += b0;
                const u32 inb = (u32)(s * NH + h) * WI;
                for (u32 j = b0 + lane; j < b1; j += 64) {
                    const u32x4 un = __builtin_amdgcn_raw_buffer_load_b128(rR, j * 16, 0, AUX_SC1);
#pragma unroll
                    for (int k = 0; k < 3; k++) {
                        const u32 Pk = un[k];
                        if (Pk == 0) continue;
                        u32 F = (((u32)((int)Pk >> 3)) & 0x8f000000u) | 0x30000000u | (((un[3] >> (8 * k)) & 0xffu) << 16);
                        if ((F & 0x7fffffffu) == 0x30000000u) F &= 0x80000000u;
                        if ((F & 0x7fffffffu) > 0x3f800000u) F = 0x7fc00000u;
                        const float mm = bf2f(m2[(size_t)(inb + (Pk & 0x1fffu)) * 4 + cmp]);
                        __hip_atomic_fetch_add(LP(double, P2_ACC + (~(Pk >> 10) & 0x1fff8u)), (double)__uint_as_float(F) * (double)mm,
                                               __ATOMIC_RELAXED, __HIP_MEMORY_SCOPE_WORKGROUP);
                    }
                }
            }
            __syncthreads();
            for (u32 e = tid; e < MAXW; e += NT) {
                const u32 hd = e / WQ, el = e - hd * WQ;
                __builtin_amdgcn_raw_buffer_store_b32(__float_as_uint((float)*LP(double, P2_ACC + e * 8)), rX, ((hd * 8 + h) * WQ + el) * 16 + cmp * 4, 0, AUX_SC1);
            }
            if (tid < WQ / 2) {
                const float a = (float)*LP(double, P2_ACC + (h * WQ + tid * 2) * 8), b = (float)*LP(double, P2_ACC + (h * WQ + tid * 2) * 8 + 8);
                sm0 = f32x4{cmp == 0 ? a : sm0.x, cmp == 1 ? a : sm0.y, cmp == 2 ? a : sm0.z, cmp == 3 ? a : sm0.w};
                sm1 = f32x4{cmp == 0 ? b : sm1.x, cmp == 1 ? b : sm1.y, cmp == 2 ? b : sm1.z, cmp == 3 ? b : sm1.w};
            }
        }
        asm volatile("s_waitcnt vmcnt(0)" ::: "memory");
        __syncthreads();
    }
    if (act) {
        SCHED();
        f32x2 gs[4], hs[4];
        auto gates = [&](int j0, int j1) {
            const f32x2 d = {half_of(ed, 0), half_of(ed, 1)};
#pragma unroll
            for (int j = j0; j < j1; j++) {
                const f32x2 df = rbf16x2(d - half_of(cw[j >> 1], j));
                const f32x2 sq2 = rbf16x2(df * df);
                const f32x2 ar = rbf16x2(sq2 * -10.0f) * 1.4426950408889634f;
                const f32x2 rb = rbf16x2(f32x2{__builtin_amdgcn_exp2f(ar.x), __builtin_amdgcn_exp2f(ar.y)});
#pragma unroll
                for (int k = 0; k < 4; k++) {
                    gs[k] += rb * half_of(wsb[j * 2 + (k >> 1)], k);
                    hs[k] += rb * half_of(wrb[j * 2 + (k >> 1)], k);
                }
            }
        };
#pragma unroll
        for (int k = 0; k < 4; k++) { gs[k] = f32x2{0.f, 0.f}; hs[k] = f32x2{0.f, 0.f}; }
        gates(0, 3);
#pragma unroll
        for (int k = 0; k < 4; k++) { PIN(gs[k]); PIN(hs[k]); }
        asm volatile("s_waitcnt vmcnt(0)" ::: "memory");
        gu32* flags = (gu32*)(ws + WS_FLAG) + (p * 16 + w) * 32;
        if (lane == 0) __hip_atomic_store(flags + h, tag + (slow ? 1u : 0u), __ATOMIC_RELAXED, __HIP_MEMORY_SCOPE_AGENT);
        gates(3, 8);
#pragma unroll
        for (int k = 0; k < 4; k++) { hs[k] = rbf16x2(hs[k]); PIN(gs[k]); PIN(hs[k]); }
#pragma unroll
        for (int k = 0; k < 8; k++) asm volatile("" : "+s"(wdn[k]), "+s"(wup[k]));
        u32 modes;
        {
            gu32* f = flags + ((h + 1 + min(lane, 6)) & 7);
            u32 v = 0;
#pragma unroll 1
            for (u32 spins = 0; spins < (1u << 22); spins++) {
                v = __hip_atomic_load(f, __ATOMIC_RELAXED, __HIP_MEMORY_SCOPE_AGENT) - tag;
                if (__all(v <= 1u)) break;
                __builtin_amdgcn_s_sleep(10);
            }
            modes = (u32)__ballot(v == 1u) & 0x7fu;
            asm volatile("" ::: "memory");
        }
        if (tid * 2 < ne) {
            auto unb = [&](u32x4 v) -> u32x4 {
                const u32x2 a = __builtin_bit_cast(u32x2, __builtin_bit_cast(u64, u32x2{v.x, v.y}) + 0x2000200020002000ull);
                const u32x2 b = __builtin_bit_cast(u32x2, __builtin_bit_cast(u64, u32x2{v.z, v.w}) + 0x2000200020002000ull);
                return u32x4{a.x, a.y, b.x, b.y};
            };
            own = unb(own);
            u32x4 lo = own & 0xffffu, hi = own >> 16;
            if (modes == 0) {
                u32x4 x[NH - 1];
#pragma unroll
                for (int q = 1; q < NH; q++)
                    x[q - 1] = __builtin_amdgcn_raw_buffer_load_b128(rY, ((h * 8 + ((h + q) & 7)) * WQ + tid * 2) * 8, 0, AUX_SC1);
#pragma unroll
                for (int q = 0; q < NH - 1; q++) { const u32x4 y = unb(x[q]); lo += y & 0xffffu; hi += y >> 16; }
            } else {
                for (int q = 1; q < NH; q++) {
                    const u32 idx = (h * 8 + ((h + q) & 7)) * WQ + tid * 2;
                    u32x4 x = {ABIAS, ABIAS, ABIAS, ABIAS};
                    if ((modes >> (q - 1)) & 1) {
                        sm0 += __builtin_bit_cast(f32x4, __builtin_amdgcn_raw_buffer_load_b128(rX, idx * 16, 0, AUX_SC1));
                        sm1 += __builtin_bit_cast(f32x4, __builtin_amdgcn_raw_buffer_load_b128(rX, idx * 16 + 16, 0, AUX_SC1));
                    } else x = __builtin_amdgcn_raw_buffer_load_b128(rY, idx * 8, 0, AUX_SC1);
                    x = unb(x);
                    lo += x & 0xffffu; hi += x >> 16;
                }
            }
            lo -= NH * (AB16 + 0x2000u); hi -= NH * (AB16 + 0x2000u);
            if (modes != 0x7f || !slow) {
                sm0 += f32x4{(float)(int)lo.x, (float)(int)hi.x, (float)(int)lo.y, (float)(int)hi.y} * quantum;
                sm1 += f32x4{(float)(int)lo.z, (float)(int)hi.z, (float)(int)lo.w, (float)(int)hi.w} * quantum;
            }
            f32x2 agg[4], o[4];
#pragma unroll
            for (int k = 0; k < 4; k++) {
                f32x2 v = {0.f, 0.f};
#pragma unroll
                for (int j = 0; j < 4; j++) v += f32x2{sm0[j], sm1[j]} * half_of(wdn[j * 2 + (k >> 1)], k);
                agg[k] = rbf16x2(v * gs[k]);
            }
#pragma unroll
            for (int k = 0; k < 4; k++) {
                f32x2 v = {0.f, 0.f};
#pragma unroll
                for (int j = 0; j < 4; j++) v += agg[j] * half_of(wup[j * 2 + (k >> 1)], k);
                o[k] = f32x2{half_of(em[k >> 1], k), half_of(em[2 + (k >> 1)], k)} + rbf16x2(rbf16x2(v) * hs[k]);
            }
            const u32x4 ov = {pk_bf16(o[0].x, o[1].x), pk_bf16(o[2].x, o[3].x), pk_bf16(o[0].y, o[1].y), pk_bf16(o[2].y, o[3].y)};
            __builtin_amdgcn_raw_buffer_store_b128(ov, RSRC(out, E * 8), (e0 + tid * 2) * 8, 0, AUX_SC1 | 1);
        }
    }
    asm volatile(".Lke%c0:" :: "i"((int)FULL));
}

__global__ void __launch_bounds__(256)
gemnet_scatter_kernel(const u16* __restrict__ m, const int* __restrict__ idx_in, const int* __restrict__ idx_out,
                      const u16* __restrict__ angle, const u16* __restrict__ ang_freq, float* acc, int T, int De, int L)
{
    const long long g = (long long)blockIdx.x * 256 + threadIdx.x;
    const int t = (int)(g / De), k = (int)(g - (long long)t * De);
    if (t >= T) return;
    const float mv = bf2f(m[(size_t)idx_in[t] * De + k]), a = bf2f(angle[t]);
    for (int l = 0; l < L; l++)
        atomicAdd(&acc[((size_t)idx_out[t] * L + l) * De + k], rbf16(cosf(rbf16(bf2f(ang_freq[l]) * a))) * mv);
}
__global__ void __launch_bounds__(256)
gemnet_edge_kernel(const u16* __restrict__ m, const u16* __restrict__ dist, const u16* __restrict__ centers,
                   const u16* __restrict__ w_rbf, const u16* __restrict__ w_sbf, const u16* __restrict__ w_down,
                   const u16* __restrict__ w_up, const float* __restrict__ acc, u16* __restrict__ out, int E, int De, int NR, int L)
{
    const long long g = (long long)blockIdx.x * 256 + threadIdx.x;
    const int e = (int)(g / De), k = (int)(g - (long long)e * De);
    if (e >= E) return;
    const float d = bf2f(dist[e]);
    auto rbf = [&](int r) { const float df = rbf16(d - bf2f(centers[r])); return rbf16(expf(rbf16(-10.0f * rbf16(df * df)))); };
    float hk = 0.f, up = 0.f;
    for (int r = 0; r < NR; r++) hk += rbf(r) * bf2f(w_rbf[r * De + k]);
    for (int j = 0; j < De; j++) {
        float agg = 0.f;
        for (int l = 0; l < L; l++) {
            float gl = 0.f, v = 0.f;
            for (int r = 0; r < NR; r++) gl += rbf(r) * bf2f(w_sbf[(r * L + l) * De + j]);
            for (int i = 0; i < De; i++) v += acc[((size_t)e * L + l) * De + i] * bf2f(w_down[i * De + j]);
            agg += v * gl;
        }
        up += rbf16(agg) * bf2f(w_up[j * De + k]);
    }
    const float o = bf2f(m[(size_t)e * De + k]) + rbf16(rbf16(up) * rbf16(hk));
    out[(size_t)e * De + k] = (u16)pk_bf16(o, o);
}

extern "C" void kernel_launch(void* const* d_in, const int* in_sizes, int n_in,
                              void* d_out, int out_size, void* d_ws, size_t ws_size,
                              hipStream_t stream) {
    const int E = in_sizes[3], T = in_sizes[1], NR = in_sizes[5], L = in_sizes[6];
    const int De = in_sizes[0] / E;
    const u32 W = (((u32)E + 31) / 32 + 15) & ~15u;
    const u32 WI = (((u32)E + NH * NS - 1) / (NH * NS) + 15) & ~15u;
    const u64 magW = (0x100000000ull + W - 1) / W, magI = (0x100000000ull + WI - 1) / WI;
    const int T4 = T / 4;
    const int RF = T4 / (NWG * NT);
    const int RQ = (T4 - RF * NWG * NT + NWG - 1) / NWG;
    int dev = 0, cus = 0;
    (void)hipGetDevice(&dev);
    (void)hipDeviceGetAttribute(&cus, hipDeviceAttributeMultiprocessorCount, dev);
    const bool fused = De == 4 && NR == 8 && L == 1 && E % 2 == 0 && T % 4 == 0 && RF < NQ && W <= (u32)MAXW && WI <= (u32)MAXWI &&
                       magW < (1u << 24) && magI < (1u << 24) &&
                       (u64)E * (magW * W - 0x100000000ull) < 0x100000000ull && (u64)E * (magI * WI - 0x100000000ull) < 0x100000000ull &&
                       ws_size >= WS_END && cus >= NWG;
    if (fused) {
        const float rfac = 1.2f * (7.0f + 4.5f * sqrtf((float)T / ((float)E * NH))) / (float)E;
        auto kf = RF == NQ - 1 ? gemnet_fused_kernel<true> : gemnet_fused_kernel<false>;
        (void)hipFuncSetAttribute((const void*)kf, hipFuncAttributeMaxDynamicSharedMemorySize, LDS_BYTES);
        kf<<<NWG, NT, LDS_BYTES, stream>>>((const u32x4*)d_in[1], (const u32x4*)d_in[2], (const u32x2*)d_in[4],
            (const u16*)d_in[6], (const u32x4*)d_in[0], (char*)d_ws, (const u16*)d_in[3],
            (const u32*)d_in[5], (const u32*)d_in[7], (const u32*)d_in[8], (const u32*)d_in[9], (const u32*)d_in[10],
            (u32x2*)d_out, T4, RF, RQ, E, W, (u32)magW, WI, (u32)magI, rfac);
    } else {
        (void)hipMemsetAsync(d_ws, 0, (size_t)E * L * De * 4, stream);
        gemnet_scatter_kernel<<<(unsigned)(((long long)T * De + 255) / 256), 256, 0, stream>>>((const u16*)d_in[0], (const int*)d_in[1],
            (const int*)d_in[2], (const u16*)d_in[4], (const u16*)d_in[6], (float*)d_ws, T, De, L);
        gemnet_edge_kernel<<<(unsigned)(((long long)E * De + 255) / 256), 256, 0, stream>>>((const u16*)d_in[0], (const u16*)d_in[3],
            (const u16*)d_in[5], (const u16*)d_in[7], (const u16*)d_in[8], (const u16*)d_in[9], (const u16*)d_in[10],
            (const float*)d_ws, (u16*)d_out, E, De, NR, L);
    }
}
```

```cpp
#include <hip/hip_runtime.h>
#include <hip/hip_bf16.h>
#include <stdint.h>
#include <math.h>

typedef unsigned short u16;
typedef unsigned int u32;
typedef unsigned long long u64;
typedef u32 u32x4 __attribute__((ext_vector_type(4)));
typedef u32 u32x2 __attribute__((ext_vector_type(2)));
typedef float f32x4 __attribute__((ext_vector_type(4)));
typedef float f32x2 __attribute__((ext_vector_type(2)));
typedef __bf16 bf16x2 __attribute__((ext_vector_type(2)));
typedef short s16x4 __attribute__((ext_vector_type(4)));
typedef __attribute__((address_space(1))) u32 gu32;

#pragma clang diagnostic ignored "-Wint-to-pointer-cast"
#define PIN(x) asm volatile("" : "+v"(x))
#define LP(T, a) ((__attribute__((address_space(3))) T*)(u32)(a))
#define SCHED() __builtin_amdgcn_sched_barrier(0)
#define RSRC(p, bytes) __builtin_amdgcn_make_buffer_rsrc((void*)(p), 0, (bytes), 0x00020000)
#define AUX_NT 2
#define AUX_SC1 16

constexpr int NT = 1024;
constexpr int NWG = 256;
constexpr int NS = 7;
constexpr int NH = 8;
constexpr int NBKT = NWG * NS;
constexpr int NQ = 13;
constexpr int WQ = 1568;
constexpr int MAXW = NH * WQ;
constexpr int MAXWI = 7168;
constexpr int NSUB = 2 * NWG;
constexpr int MAXN = 7 * 4096;
constexpr int UMAX = (MAXN + 2 * NBKT) / 3 + 1;
constexpr int UPC = 10760;
constexpr int LDS_BYTES = 163840;
constexpr int P1_DUMMY = NBKT * 4;
constexpr int P1_SCR = P1_DUMMY + 256;
constexpr int P1_STG = P1_SCR + 256;
constexpr int P1_UA = (LDS_BYTES - P1_STG) / 16;
constexpr int ND = (P1_UA + NT - 1) / NT;
static_assert(UMAX < UPC && 3 * UMAX < 65535 && P1_UA % 32 == 0 && ND == 10, "geometry");
constexpr int P2_ACC = LDS_BYTES - MAXW * 8;
constexpr int LCAP = 3 * NT;
constexpr int P2_HEAD = LDS_BYTES - NS * LCAP * 4;
constexpr int P2_SCR = MAXWI * 8;
constexpr u32 AB16 = 0x4000u;
constexpr u32 ABIAS = AB16 * 0x10001u;
static_assert(P2_SCR == NT * 56 && P2_SCR + 1024 <= P2_ACC && P2_ACC <= P2_HEAD && P2_ACC < 65536 && P2_ACC + 0x1fff8 >= LDS_BYTES, "layout");

constexpr size_t WS_REC = 0;
constexpr size_t WS_OFFS = WS_REC + (size_t)NSUB * UPC * 16;
constexpr size_t WS_OFFX = WS_OFFS + (size_t)NSUB * 256 * 32;
constexpr size_t WS_STAT = WS_OFFX + (size_t)NSUB * 256 * 32;
constexpr size_t WS_EPOCH = WS_STAT + 4096;
constexpr size_t WS_PHASE = WS_EPOCH + 4096;
constexpr size_t WS_FLAG = WS_PHASE + 8 * 1024;
constexpr size_t WS_PART = WS_FLAG + 32 * 16 * 128;
constexpr size_t WS_PART16 = WS_PART + (size_t)32 * 64 * WQ * 16;
constexpr size_t WS_END = WS_PART16 + (size_t)32 * 64 * WQ * 8;

__device__ __forceinline__ float bf2f(u16 v) { return __uint_as_float(((u32)v) << 16); }
__device__ __forceinline__ u32 pk_bf16(float a, float b) {
    f32x2 v = {a, b};
    bf16x2 r = __builtin_convertvector(v, bf16x2);
    return __builtin_bit_cast(u32, r);
}
__device__ __forceinline__ float rbf16(float v) { return __uint_as_float(pk_bf16(v, v) << 16); }
__device__ __forceinline__ f32x2 rbf16x2(f32x2 v) {
    const u32 t = pk_bf16(v.x, v.y);
    return f32x2{__uint_as_float(t << 16), __uint_as_float(t & 0xffff0000u)};
}
template <int CTRL, int RM>
__device__ __forceinline__ u32 dpp0(u32 v) { return __builtin_amdgcn_update_dpp(0u, v, CTRL, RM, 0xf, false); }
__device__ __forceinline__ u32 row_incl_sum(u32 v) {
    v += dpp0<0x111, 0xf>(v);
    v += dpp0<0x112, 0xf>(v);
    v += dpp0<0x114, 0xf>(v);
    v += dpp0<0x118, 0xf>(v);
    return v;
}
__device__ __forceinline__ u32 half_incl_sum(u32 v) {
    v = row_incl_sum(v);
    return v + dpp0<0x142, 0xa>(v);
}
__device__ __forceinline__ float wave_incl_sumf(float v) {
    v += __uint_as_float(dpp0<0x111, 0xf>(__float_as_uint(v)));
    v += __uint_as_float(dpp0<0x112, 0xf>(__float_as_uint(v)));
    v += __uint_as_float(dpp0<0x114, 0xf>(__float_as_uint(v)));
    v += __uint_as_float(dpp0<0x118, 0xf>(__float_as_uint(v)));
    v += __uint_as_float(dpp0<0x142, 0xa>(__float_as_uint(v)));
    v += __uint_as_float(dpp0<0x143, 0xc>(__float_as_uint(v)));
    return v;
}
__device__ __forceinline__ u32 wave_incl_max(u32 v) {
    v = max(v, dpp0<0x111, 0xf>(v));
    v = max(v, dpp0<0x112, 0xf>(v));
    v = max(v, dpp0<0x114, 0xf>(v));
    v = max(v, dpp0<0x118, 0xf>(v));
    v = max(v, dpp0<0x142, 0xa>(v));
    v = max(v, dpp0<0x143, 0xc>(v));
    return v;
}
__device__ __forceinline__ u32 mulhi24(u32 v, u32 s) { u32 r; asm("v_mul_hi_u32_u24 %0, %1, %2" : "=v"(r) : "s"(s), "v"(v)); return r; }
__device__ __forceinline__ u32 mad24(u32 a, int s, u32 c) { u32 r; asm("v_mad_i32_i24 %0, %1, %2, %3" : "=v"(r) : "v"(a), "s"(s), "v"(c)); return r; }
__device__ __forceinline__ u32 bsel(u32 m, u32 a, u32 b) { return __builtin_amdgcn_bitop3_b32(m, a, b, 0xca); }
__device__ __forceinline__ u32 andor(u32 a, u32 m, u32 b) { return __builtin_amdgcn_bitop3_b32(a, m, b, 0xea); }
__device__ __forceinline__ u32 or3(u32 a, u32 b, u32 c) { return __builtin_amdgcn_bitop3_b32(a, b, c, 0xfe); }
__device__ __forceinline__ u32 shl3_16(u32 v) { u32 r; asm("v_lshlrev_b16 %0, 3, %1" : "=v"(r) : "v"(v)); return r; }
__device__ __forceinline__ u32 shl2_16(u32 v) { u32 r; asm("v_lshlrev_b16 %0, 2, %1" : "=v"(r) : "v"(v)); return r; }
__device__ __forceinline__ u32 div3(u32 v) { return (u32)__umul24(v, 43691u) >> 17; }
__device__ __forceinline__ u32 lds_add_rtn(u32 addr, u32 v) { return __hip_atomic_fetch_add(LP(u32, addr), v, __ATOMIC_RELAXED, __HIP_MEMORY_SCOPE_WORKGROUP); }
__device__ __forceinline__ float half_of(u32 w, int k) { return __uint_as_float((k & 1) ? (w & 0xffff0000u) : (w << 16)); }

__device__ __forceinline__ float rev_large(float x) {
    const u32 b = __float_as_uint(x) & 0x7fffffffu;
    const u32 k = min((b >> 23) - 134u, 127u), j = k >> 5;
    const u32 M = ((b >> 16) & 0x7fu) | 0x80u;
    const u32 hi = j == 0 ? 0x28be60dbu : j == 1 ? 0x9391054au : j == 2 ? 0x7f09d5f4u : 0x7d4d3770u;
    const u32 lo = j == 0 ? 0x9391054au : j == 1 ? 0x7f09d5f4u : j == 2 ? 0x7d4d3770u : 0x36d8a566u;
    const u32 f = (u32)(((((u64)hi << 32) | lo) << (k & 31u)) >> 32);
    const float r = (float)(M * f) * 2.3283064365386963e-10f;
    return b >= 0x7f800000u ? __uint_as_float(0x7fc00000u) : r;
}

template <bool FULL>
__global__ void __launch_bounds__(NT, 4)
gemnet_fused_kernel(const u32x4* __restrict__ idx_in, const u32x4* __restrict__ idx_out, const u32x2* __restrict__ angle,
                    const u16* __restrict__ ang_freq, const u32x4* __restrict__ m16, char* ws,
                    const u16* __restrict__ dist, const u32* __restrict__ centers, const u32* __restrict__ w_rbf,
                    const u32* __restrict__ w_sbf, const u32* __restrict__ w_down, const u32* __restrict__ w_up,
                    u32x2* __restrict__ out, int T4, int RF, int RQ, int E, u32 W, u32 magW, u32 WI, u32 magI, float rfac)
{
    const int tid = threadIdx.x;
    const int lane = tid & 63;
    const int w = __builtin_amdgcn_readfirstlane(tid >> 6);
    const int c = blockIdx.x;
    const int m_n16 = E >> 1;
    u32 warm = 0;
    {
        u64 kpc; u32 klen;
        asm volatile("s_getpc_b64 %0\n.Lks%c2:\n\ts_mov_b32 %1, .Lke%c2-.Lks%c2" : "=s"(kpc), "=s"(klen) : "i"((int)FULL));
        if (w == 13) warm = __builtin_amdgcn_raw_buffer_load_b32(RSRC(kpc, klen), lane < 32 ? (u32)(lane * 32 + (c >> 3)) * 128u : 0xfffffff0u, 0, 0);
        if (w == 12) {
            const u32 off = lane < 16 ? (u32)(WS_FLAG - WS_STAT) + ((c >> 3) * 16 + lane) * 128
                          : lane < 24 ? (u32)(WS_PHASE - WS_STAT) + (c & 7) * 1024 + (lane - 16) * 128 : (c >> 3) * 128;
            warm = __builtin_amdgcn_raw_buffer_load_b32(RSRC(ws + WS_STAT, (int)(WS_PART - WS_STAT)), off, 0, AUX_SC1);
        }
    }
    const __amdgpu_buffer_rsrc_t rI = RSRC(idx_in, T4 * 16), rO = RSRC(idx_out, T4 * 16), rA = RSRC(angle, T4 * 8);
    const int rem0 = RF * NWG * NT + c * RQ;
    const bool valid0 = tid < min(RQ, T4 - rem0);
    auto skip = [&](int i) -> bool { return i == 0 ? w * 64 >= RQ : (!FULL && i - 1 >= RF); };
    const int mper = (m_n16 + NWG - 1) / NWG;
    const int m0i = min(c * mper, m_n16);
    const __amdgpu_buffer_rsrc_t rT = RSRC(ws + WS_OFFS, (int)(WS_FLAG - WS_OFFS));

    u32x4 rin[NQ], rout[NQ];
    u32x2 rang[NQ];
    u32 P[NQ][4], B8[NQ], BK[NQ][2];

    const u32 t16 = tid * 16, t8 = tid * 8;
    const u32 epoch = __builtin_amdgcn_raw_buffer_load_b32(rT, (int)(WS_EPOCH - WS_OFFS), 0, AUX_SC1);
    const u32x4 mv = __builtin_amdgcn_raw_buffer_load_b128(RSRC(m16 + m0i, min(mper, m_n16 - m0i) * 16), t16, 0, 0);
    auto issue = [&](int i) {
        if (i == 0) {
            const u32 q = valid0 ? rem0 + tid : 0x0fffffffu;
            rin[i] = __builtin_amdgcn_raw_buffer_load_b128(rI, q * 16, 0, AUX_NT);
            rout[i] = __builtin_amdgcn_raw_buffer_load_b128(rO, q * 16, 0, AUX_NT);
            rang[i] = __builtin_amdgcn_raw_buffer_load_b64(rA, q * 8, 0, AUX_NT);
        } else if (!skip(i)) {
            const u32 qb = ((i - 1) * NWG + c) * NT;
            rin[i] = __builtin_amdgcn_raw_buffer_load_b128(rI, t16, qb * 16, AUX_NT);
            rout[i] = __builtin_amdgcn_raw_buffer_load_b128(rO, t16, qb * 16, AUX_NT);
            rang[i] = __builtin_amdgcn_raw_buffer_load_b64(rA, t8, qb * 8, AUX_NT);
        }
    };
    issue(0); issue(1);
    const float freq = bf2f(ang_freq[0]);
    SCHED();
    const u32x4 z4 = {0, 0, 0, 0};
#pragma unroll
    for (int k = 0; k < LDS_BYTES / 16 / NT; k++) *LP(u32x4, t16 + k * NT * 16) = z4;
    __syncthreads();
    asm volatile("" :: "v"(warm));
    const u32 tag = __builtin_amdgcn_readfirstlane(epoch) + 0x9e3779b9u;
    {
        u32 lo[4], hi[4], mx = 0;
#pragma unroll
        for (int e = 0; e < 4; e++) { lo[e] = (mv[e] << 16) & 0x7fffffffu; hi[e] = mv[e] & 0x7fff0000u; mx = max(mx, max(lo[e], hi[e])); }
        mx = wave_incl_max(mx);
        f32x4 sa = {__uint_as_float(lo[0]) + __uint_as_float(lo[2]), __uint_as_float(hi[0]) + __uint_as_float(hi[2]),
                    __uint_as_float(lo[1]) + __uint_as_float(lo[3]), __uint_as_float(hi[1]) + __uint_as_float(hi[3])};
#pragma unroll
        for (int e = 0; e < 4; e++) sa[e] = wave_incl_sumf(sa[e]);
        if (lane == 63) {
            __hip_atomic_fetch_max(LP(u32, P1_SCR + 64), mx, __ATOMIC_RELAXED, __HIP_MEMORY_SCOPE_WORKGROUP);
            *LP(float, P1_SCR + 128 + w * 4) = fmaxf(fmaxf(sa[0], sa[1]), fmaxf(sa[2], sa[3]));
        }
    }
    const bool unit_freq = __builtin_amdgcn_readfirstlane(__float_as_uint(freq)) == 0x3f800000u;
    u32 nanacc = 0;
    u32 vm8 = 0x80008000u, vmt = 0xf8000000u, v12 = 12;
    PIN(vm8); PIN(vmt); PIN(v12);

    auto encode = [&](int i) {
        if (skip(i)) return;
        u32 x01 = rang[i].x, x23 = rang[i].y;
        if (!unit_freq) {
            x01 = pk_bf16(half_of(x01, 0) * freq, half_of(x01, 1) * freq);
            x23 = pk_bf16(half_of(x23, 0) * freq, half_of(x23, 1) * freq);
        }
        const float x[4] = {half_of(x01, 0), half_of(x01, 1), half_of(x23, 0), half_of(x23, 1)};
        float r[4];
#pragma unroll
        for (int k = 0; k < 4; k++) r[k] = x[k] * 0.15915494309189535f;
        const float rm = fmaxf(fmaxf(fabsf(r[0]), fabsf(r[1])), fmaxf(fabsf(r[2]), fabsf(r[3])));
        if (__builtin_expect(__any(rm >= 20.0f), 0)) {
#pragma unroll
            for (int k = 0; k < 4; k++) r[k] = fabsf(x[k]) >= 128.0f ? rev_large(x[k]) : r[k];
        }
        const u32 c01 = pk_bf16(__builtin_amdgcn_cosf(r[0]), __builtin_amdgcn_cosf(r[1]));
        const u32 c23 = pk_bf16(__builtin_amdgcn_cosf(r[2]), __builtin_amdgcn_cosf(r[3]));
        nanacc = or3(c01, c23, nanacc);
        PIN(nanacc);
        B8[i] = __builtin_amdgcn_perm(c23, c01, 0x06040200u);
        const u32 t01 = bsel(vm8, c01, c01 << 3), t23 = bsel(vm8, c23, c23 << 3);
        const u32 F[4] = {t01 << 16, t01, t23 << 16, t23};
        u32 bk[4];
#pragma unroll
        for (int k = 0; k < 4; k++) {
            const u32 vi = rin[i][k], vo = rout[i][k];
            const u32 q = mulhi24(vi, magI);
            const u32 p = mulhi24(vo, magW);
            const u32 li = mad24(q, -(int)WI, vi);
            const u32 lo = mad24(p, -(int)W, vo);
            P[i][k] = bsel(vmt, F[k], ((lo << 13) + li) ^ 0x07ffe000u);
            bk[k] = shl2_16((q << 5) + p);
            if (i == 0) bk[k] = valid0 ? bk[k] : (u32)(P1_DUMMY + lane * 4);
            lds_add_rtn(bk[k], i < 7 ? 1u : 0x10000u);
        }
        BK[i][0] = bk[0] | (bk[1] << 16);
        BK[i][1] = bk[2] | (bk[3] << 16);
        PIN(BK[i][0]); PIN(BK[i][1]); PIN(B8[i]); PIN(P[i][0]); PIN(P[i][1]); PIN(P[i][2]); PIN(P[i][3]);
    };
    const __amdgpu_buffer_rsrc_t rR = RSRC(ws + WS_REC, NSUB * UPC * 16);
    auto scan = [&](int sub) -> u32 {
        u32 un[7], st[7];
        if (w < 4) {
#pragma unroll
            for (int k = 0; k < 7; k++) un[k] = div3((*LP(u32, tid * 4 + k * 1024) >> (16 * sub)) + 2);
            u32 run = 0;
#pragma unroll
            for (int j = 0; j < 4; j++) {
                const u32 v = half_incl_sum(un[2 * j] | (j < 3 ? un[2 * j + 1] << 16 : 0u));
                const u32 t0 = __builtin_amdgcn_readlane(v, 31), t1 = __builtin_amdgcn_readlane(v, 63);
                const u32 tt = lane < 32 ? t0 : t1;
                st[2 * j] = run + (v & 0xffffu) - un[2 * j];
                run += tt & 0xffffu;
                if (j < 3) { st[2 * j + 1] = run + (v >> 16) - un[2 * j + 1]; run += tt >> 16; }
            }
            if ((lane & 31) == 0) *LP(u32, P1_SCR + (tid >> 5) * 4) = run;
        }
        if (w == 8) *LP(u32, P1_DUMMY + lane * 4) = 0xffffu;
        __syncthreads();
        const u32 ht = *LP(u32, P1_SCR + (lane & 7) * 4);
        u32 hs = ht + dpp0<0x111, 0xf>(ht);
        hs += dpp0<0x112, 0xf>(hs); hs += dpp0<0x114, 0xf>(hs);
        const u32 U = __builtin_amdgcn_readlane(hs, 7);
        if (w < 4) {
            const u32 b0 = __builtin_amdgcn_readlane(hs - ht, 2 * w), b1 = __builtin_amdgcn_readlane(hs - ht, 2 * w + 1);
            const u32 base = lane < 32 ? b0 : b1;
            u32 nn[7], dd[7], sat = *LP(u32, P1_SCR + 96);
#pragma unroll
            for (int k = 0; k < 7; k++) {
                st[k] += base;
                *LP(u32, tid * 4 + k * 1024) = st[k] * 3;
                nn[k] = min(un[k], 31u);
                dd[k] = k ? min(st[k] - st[k - 1], 1023u) : 0u;
                sat |= (nn[k] == 31u) | (k != 4 && dd[k] == 1023u);
            }
            if (sat) nn[0] = 31u;
            const u32 x0 = st[0] | (dd[1] << 14) | (nn[0] << 24) | (nn[3] << 29), z0 = dd[2] | (dd[3] << 10) | (nn[1] << 20) | (nn[2] << 25) | ((nn[3] >> 3) << 30);
            const u32 x1 = st[4] | (dd[5] << 14) | (nn[4] << 24), z1 = dd[6] | (nn[5] << 20) | (nn[6] << 25);
#pragma unroll
            for (int j = 0; j < 2; j++) {
                const int src = (j * 32 + (lane >> 1)) * 4;
                const u32 a0 = __builtin_amdgcn_ds_bpermute(src, x0), a1 = __builtin_amdgcn_ds_bpermute(src, x1);
                const u32 c0 = __builtin_amdgcn_ds_bpermute(src, z0), c1 = __builtin_amdgcn_ds_bpermute(src, z1);
                const u32x4 o = {(lane & 1) ? a1 : a0, tag, (lane & 1) ? c1 : c0, tag};
                __builtin_amdgcn_raw_buffer_store_b128(o, rT, (sub * NWG + c) * 8192 + w * 2048 + j * 1024 + lane * 16, 0, AUX_SC1);
            }
            if (sat) {
#pragma unroll
                for (int k = 0; k < 7; k++)
                    __builtin_amdgcn_raw_buffer_store_b32(st[k] | (un[k] << 16), rT, (int)(WS_OFFX - WS_OFFS) + (sub * NWG + c) * 8192 + tid * 32 + k * 4, 0, AUX_SC1);
            }
        }
        if (U > (u32)P1_UA) {
            for (u32 u = P1_UA + tid; u < U; u += NT) __builtin_amdgcn_raw_buffer_store_b128(z4, rR, ((sub * NWG + c) * UPC + u) * 16, 0, AUX_SC1);
            asm volatile("s_waitcnt vmcnt(0)" ::: "memory");
        }
        __syncthreads();
        return U;
    };
    u32 pos[NQ][4];
    auto cursor = [&](int i) {
        if (skip(i)) return;
        PIN(BK[i][0]); PIN(BK[i][1]);
        const u32 inc = i > 0 || valid0 ? 1u : 0u;
        pos[i][0] = lds_add_rtn(BK[i][0] & 0xffffu, inc); pos[i][1] = lds_add_rtn(BK[i][0] >> 16, inc);
        pos[i][2] = lds_add_rtn(BK[i][1] & 0xffffu, inc); pos[i][3] = lds_add_rtn(BK[i][1] >> 16, inc);
    };
    auto scatter = [&](int i, bool big) {
        if (skip(i)) return;
#pragma unroll
        for (int k = 0; k < 4; k++) {
            const u32 ps = i < 7 ? (pos[i][k] & 0xffffu) : pos[i][k];
            const u32 t = div3(ps), zz = t >> 1;
            *LP(u32, __builtin_amdgcn_bitop3_b32((ps + t) << 2, zz, v12, 0x78) + P1_STG) = P[i][k];
            const u32 src = (k & 1) ? (B8[i] >> 8) : B8[i];
            *LP(unsigned char, __builtin_amdgcn_bitop3_b32(mad24(t, 13, ps), zz, v12, 0xf2) + P1_STG) = (unsigned char)((k & 2) ? (src >> 16) : src);
        }
        if (big) {
            const u32 g = ((i >= 7) * NWG + c) * UPC * 16;
#pragma unroll
            for (int k = 0; k < 4; k++) {
                const u32 ps = pos[i][k] & 0xffffu, t = div3(ps);
                if (t >= (u32)P1_UA && ps != 0xffffu) {
                    __builtin_amdgcn_raw_buffer_store_b32(P[i][k], rR, g + (ps + t) * 4, 0, AUX_SC1);
                    __builtin_amdgcn_raw_buffer_store_b8((unsigned char)(B8[i] >> (8 * k)), rR, g + 13 * t + ps + 12, 0, AUX_SC1);
                }
            }
        }
    };
    const u32 sw1 = (tid & 8) ? ~0u : 0u, sw2 = (tid & 16) ? ~0u : 0u;
    auto unswz = [&](u32x4 v) -> u32x4 {
        const u32x4 a = {bsel(sw1, v.y, v.x), bsel(sw1, v.x, v.y), bsel(sw1, v.w, v.z), bsel(sw1, v.z, v.w)};
        return u32x4{bsel(sw2, a.z, a.x), bsel(sw2, a.w, a.y), bsel(sw2, a.x, a.z), bsel(sw2, a.y, a.w)};
    };

    auto piece = [&](int k) -> u32 {
        u32 r;
        asm volatile("v_add_u32 %0, %1, %2" : "=v"(r) : "i"(k * NT * 16), "v"(t16));
        return r;
    };

#pragma unroll
    for (int i = 0; i < 7; i++) {
        issue(i + 2);
        SCHED();
        encode(i);
        SCHED();
    }
    __syncthreads();
    const u32 UA_ = scan(0);
    if (tid == 0) {
        float t = 0.f;
#pragma unroll
        for (int k = 0; k < 4; k++) { const f32x4 v = *LP(f32x4, P1_SCR + 128 + k * 16); t += (v.x + v.y) + (v.z + v.w); }
        __builtin_amdgcn_raw_buffer_store_b128(u32x4{*LP(u32, P1_SCR + 64), tag, __float_as_uint(t), tag}, rT, (int)(WS_STAT - WS_OFFS) + c * 16, 0, AUX_SC1);
    }
    {
        const bool big = UA_ > (u32)P1_UA;
        cursor(0);
#pragma unroll
        for (int i = 0; i < 7; i++) {
            if (i + 1 < 7) cursor(i + 1);
            SCHED();
            scatter(i, big);
            SCHED();
            if (i & 1) encode(7 + i / 2);
            else issue(9 + i / 2);
            SCHED();
        }
    }
    __syncthreads();
    u32x4 DR[ND];
#pragma unroll
    for (int k = 0; k < ND; k++) {
        DR[k] = *LP(u32x4, P1_STG + t16 + k * NT * 16);
        *LP(u32x4, P1_STG + t16 + k * NT * 16) = z4;
    }
    SCHED();
    const __amdgpu_buffer_rsrc_t rS0 = RSRC(ws + WS_REC + (size_t)(u32)(c * UPC) * 16u, min((UA_ + 7) & ~7u, (u32)P1_UA) * 16);
    const int h = c & 7, p = c >> 3;
    const int task = h * 32 + p;
    const bool sg = lane >= 32;
    const int cc = w * 32 + (lane & 31);
    u32x4 o4 = {0, 0, 0, 0}, mxv = {0, tag, 0, tag};
    auto load_tables = [&]() {
        o4 = __builtin_amdgcn_raw_buffer_load_b128(rT, cc * 8192 + task * 32 + (sg ? 16 : 0), 0, AUX_SC1);
        if (w < 4) mxv = __builtin_amdgcn_raw_buffer_load_b128(rT, (int)(WS_STAT - WS_OFFS) + t16, 0, AUX_SC1);
        SCHED();
    };
    encode(10); SCHED();
    encode(11); SCHED();
    encode(12); SCHED();
    if (nanacc & 0x40004000u) *LP(u32, P1_SCR + 96) = 1;
    __syncthreads();
    const u32 UB_ = scan(1);
    if (FULL && w < 8) load_tables();
    {
        const bool big = UB_ > (u32)P1_UA;
        cursor(7);
#pragma unroll
        for (int i = 7; i < NQ; i++) {
            if (i + 1 < NQ) cursor(i + 1);
            SCHED();
#pragma unroll
            for (int k = 2 * (i - 8); k < 2 * (i - 7); k++)
                if (k >= 0) { __builtin_amdgcn_raw_buffer_store_b128(unswz(DR[k]), rS0, piece(k), 0, AUX_SC1); SCHED(); }
            scatter(i, big);
            SCHED();
        }
    }
    __syncthreads();

#pragma unroll
    for (int k = 0; k < ND; k++) DR[k] = *LP(u32x4, P1_STG + t16 + k * NT * 16);
    const __amdgpu_buffer_rsrc_t rS1 = RSRC(ws + WS_REC + (size_t)(u32)((NWG + c) * UPC) * 16u, min((UB_ + 7) & ~7u, (u32)P1_UA) * 16);
    auto stores = [&](int k0, int k1) {
#pragma unroll
        for (int k = k0; k < k1; k++) { __builtin_amdgcn_raw_buffer_store_b128(unswz(DR[k]), rS1, piece(k), 0, AUX_SC1); SCHED(); }
    };
    auto arrive = [&](int ad) { asm volatile("" ::: "memory"); SCHED(); if (lane == 0) lds_add_rtn(ad, 1u); SCHED(); };
    auto await = [&](int ad) {
        SCHED();
        while (__builtin_amdgcn_readfirstlane(__hip_atomic_load(LP(u32, ad), __ATOMIC_RELAXED, __HIP_MEMORY_SCOPE_WORKGROUP)) < 16u)
            __builtin_amdgcn_s_sleep(2);
        asm volatile("" ::: "memory");
        SCHED();
    };
    if (!FULL || w >= 8) load_tables();
    stores(0, 1);
    __syncthreads();
#pragma unroll
    for (int k = 0; k < 5; k++) *LP(u32x4, P2_HEAD + t16 + k * NT * 16) = z4;
    if (w < 4) *LP(u32x4, P2_HEAD + t16 + 5 * NT * 16) = z4;
    if (tid == 0) *LP(u32, P2_SCR + 400) = 0;
    if (tid < 16) *LP(u32, P2_SCR + 448 + tid * 4) = 0;
    SCHED();
#pragma unroll 1
    for (u32 spins = 0; spins < (1u << 20) && !__all(o4.y == tag && o4.w == tag && mxv.y == tag && mxv.w == tag); spins++) {
        asm volatile("" ::: "memory");
        load_tables();
    }
    u32 n[4], inc[4], st[4];
    n[0] = (o4.x >> 24) & 31u; n[1] = (o4.z >> 20) & 31u; n[2] = (o4.z >> 25) & 31u; n[3] = (o4.x >> 29) | ((o4.z >> 30) << 3);
    st[0] = o4.x & 0x3fffu; st[1] = st[0] + ((o4.x >> 14) & 1023u); st[2] = st[1] + (o4.z & 1023u); st[3] = st[2] + ((o4.z >> 10) & 1023u);
#pragma unroll
    for (int k = 0; k < 4; k++) inc[k] = half_incl_sum(k == 0 && n[0] == 31u ? (u32)LCAP + 1 : n[k]);
    if ((lane & 31) == 31) {
#pragma unroll
        for (int k = 0; k < 4; k++) *LP(u32, P2_SCR + 512 + ((sg * 4 + k) * 16 + w) * 4) = inc[k];
    }
    if (w < 4) {
        const u32 mx = wave_incl_max(mxv.x);
        const float sm = wave_incl_sumf(__uint_as_float(mxv.z));
        if (lane == 63) { *LP(u32, P2_SCR + 384 + w * 4) = mx; *LP(float, P2_SCR + 416 + w * 4) = sm; }
    }
    arrive(P1_SCR + 72);
    stores(1, 4);
    await(P1_SCR + 72);
    u64 m3;
    bool slow;
    {
        const u32 ta = *LP(u32, P2_SCR + 512 + lane * 4), tb = *LP(u32, P2_SCR + 768 + lane * 4);
        u32 ia = row_incl_sum(ta), ib = row_incl_sum(tb);
        slow = __any(max(ia, ib) > (u32)LCAP);
        m3 = (__ballot(ia > (u32)(2 * NT + w * 64)) & 0x8000800080008000ull) | ((__ballot(ib > (u32)(2 * NT + w * 64)) & 0x8000800080008000ull) >> 1);
        const u32 tt = __builtin_amdgcn_readlane(w < 4 ? ia : ib, (w & 3) * 16 + 15);
        ia -= ta; ib -= tb;
        bool more = false;
        u32 nx[4], en[4], dl[4];
        const u32 hb0 = P2_HEAD + (sg ? 4 * LCAP * 4 : 0);
#pragma unroll
        for (int k = 0; k < 4; k++) {
            const u32 ca = __builtin_amdgcn_readlane(ia, k * 16 + w), cb = __builtin_amdgcn_readlane(ib, k * 16 + w);
            const u32 carry = sg ? cb : ca;
            const u32 ex = carry + inc[k] - n[k];
            en[k] = min(carry + inc[k], (u32)LCAP);
            dl[k] = ((u32)cc * UPC + st[k] - ex) * 16;
            const u32 hb = hb0 + k * LCAP * 4;
            *LP(u32, ex < en[k] ? hb + ex * 4 : LDS_BYTES) = dl[k];
            const u32 b1 = (ex | 7u) + 1;
            *LP(u32, b1 < en[k] ? hb + b1 * 4 : LDS_BYTES) = dl[k];
            nx[k] = b1 + 8;
            more |= nx[k] < en[k];
        }
        if (more) {
#pragma unroll
            for (int k = 0; k < 4; k++)
                for (u32 ps = nx[k]; ps < en[k]; ps += 8) *LP(u32, hb0 + k * LCAP * 4 + ps * 4) = dl[k];
        }
        if (w < NS) {
            for (u32 ps = (tt & ~7u) + lane * 8; ps < (u32)LCAP; ps += 512) *LP(u32, P2_HEAD + (w * LCAP + max(ps, tt)) * 4) = 0xf0000000u;
        }
    }
    const u32x4 mq = *LP(u32x4, P2_SCR + 384);
    const u32 maxbits = __builtin_amdgcn_readfirstlane(max(max(mq[0], mq[1]), max(mq[2], mq[3])));
    const f32x4 sq = *LP(f32x4, P2_SCR + 416);
    const float rtyp = rfac * ((sq.x + sq.y) + (sq.z + sq.w));
    const float range = fmaxf(rtyp, 2.002f * __uint_as_float(maxbits));
    slow = slow || maxbits < 0x10000000u || maxbits > 0x70000000u || __any(range > 2.0f * rtyp);
    arrive(P1_SCR + 76);
    stores(4, 7);
    await(P1_SCR + 76);
    u32 hv[NS][3];
#pragma unroll
    for (int s = 0; s < NS; s++)
#pragma unroll
        for (int t = 0; t < 3; t++) hv[s][t] = *LP(u32, P2_HEAD + (s * LCAP + t * NT + tid) * 4);
    stores(7, ND);
    asm volatile("s_waitcnt vmcnt(0) lgkmcnt(0)\n\ts_barrier" ::: "memory");
    if (w == 14 && lane < 8) __builtin_amdgcn_raw_buffer_store_b32(tag, rT, (int)(WS_PHASE - WS_OFFS) + lane * 1024 + c * 4, 0, AUX_SC1);
    const __amdgpu_buffer_rsrc_t rM = RSRC(m16, m_n16 * 16);
    u32x4 wr[NS][3]; u32x2 wr3[NS];
    auto wload = [&](int j) {
        const int s = j >> 2, k = j & 3;
        const u32 base = (u32)(s * NH + h) * WI * 8;
        if (k < 3) wr[s][k] = __builtin_amdgcn_raw_buffer_load_b128(rM, base + k * 16384 + t16, 0, 0);
        else wr3[s] = __builtin_amdgcn_raw_buffer_load_b64(rM, base + 49152 + t8, 0, 0);
    };
    auto wstore = [&](int s) {
#pragma unroll
        for (int k = 0; k < 3; k++) *LP(u32x4, k * 16384 + t16) = wr[s][k];
        *LP(u32x2, 49152 + t8) = wr3[s];
    };
#pragma unroll
    for (int j = 0; j < 4; j++) wload(j);
    SCHED();
    {
        const u32x4 b4 = {ABIAS, ABIAS, ABIAS, ABIAS};
#pragma unroll
        for (int k = 0; k < 6; k++) *LP(u32x4, P2_ACC + t16 + k * NT * 16) = b4;
        if (w < 2) *LP(u32x4, P2_ACC + t16 + 6 * NT * 16) = b4;
    }
    u32 p16[3] = {t16, t16 + NT * 16, t16 + 2 * NT * 16};
    PIN(p16[1]); PIN(p16[2]);
    auto expand = [&](int s) {
#pragma unroll
        for (int t = 0; t < 3; t++) {
            u32 v = hv[s][t];
            v = max(v, dpp0<0x111, 0xf>(v));
            v = max(v, dpp0<0x112, 0xf>(v));
            v = max(v, dpp0<0x114, 0xf>(v));
            hv[s][t] = v + p16[t];
        }
#pragma unroll
        for (int t = 0; t < 3; t++) PIN(hv[s][t]);
    };
    expand(0); expand(1);
    SCHED();
    wstore(0);
    asm volatile("" :: "s"(centers[0]), "s"(w_sbf[0]), "s"(w_rbf[0]), "s"(w_down[0]), "s"(w_up[0]));

    const u32 sbits = __builtin_amdgcn_readfirstlane(__float_as_uint((float)AB16 / range)) & 0xffff0000u;
    const float quantum = __uint_as_float(__builtin_amdgcn_readfirstlane(__float_as_uint(1.0f / __uint_as_float(sbits))));
    u32 ovf = 0;
    auto handoff = [&]() {
        if (w == 15) {
#pragma unroll 1
            for (u32 spins = 0; spins < (1u << 20); spins++) {
                asm volatile("" ::: "memory");
                const u32x4 v = __builtin_amdgcn_raw_buffer_load_b128(rT, (int)(WS_PHASE - WS_OFFS) + h * 1024 + lane * 16, 0, AUX_SC1);
                if (__all(v.x == tag && v.y == tag && v.z == tag && v.w == tag)) break;
                __builtin_amdgcn_s_sleep(4);
            }
        }
        asm volatile("s_waitcnt lgkmcnt(0)\n\ts_barrier" ::: "memory");
        if (c == 0 && tid == 0) __builtin_amdgcn_raw_buffer_store_b32(tag, rT, (int)(WS_EPOCH - WS_OFFS), 0, AUX_SC1);
    };
    if (!slow) {
        u32x2 idS = {(lane & 3) == 0 ? sbits >> 16 : (lane & 3) == 1 ? sbits : 0u, (lane & 3) == 2 ? sbits >> 16 : (lane & 3) == 3 ? sbits : 0u};
        float MAG = 8421376.0f;
        u32 vmf = 0x8f000000u, v30 = 0x30000000u, vma = 0x1fff8u;
        PIN(MAG); PIN(vmf); PIN(v30); PIN(idS); PIN(vma);
        auto third = [&](int s) -> bool { return (m3 >> (16 * (s & 3) + 15 - (s >> 2))) & 1; };
        u32x4 RP[NS][3];
        u32x2 G[NS][9];
        u64 OLD[NS][9];
        constexpr int wsch[NS + 1] = {16, 18, 20, 23, 25, 28, 28, 28};
#pragma unroll
        for (int j = 4; j < wsch[0]; j++) wload(j);
        SCHED();
        handoff();
        auto rload = [&](int s, int t) {
            if (s < NS && (t < 2 || third(s))) RP[s][t] = __builtin_amdgcn_raw_buffer_load_b128(rR, hv[s][t], 0, AUX_SC1);
            SCHED();
        };
        auto gather = [&](int s, int t) {
#pragma unroll
            for (int k = 0; k < 3; k++) G[s][3 * t + k] = *LP(u32x2, shl3_16(RP[s][t][k]));
        };
        auto accum = [&](int s, int r) {
            const int t = r / 3, k = r % 3;
            const u32 Pk = RP[s][t][k];
            const f32x4 ms = __builtin_amdgcn_mfma_f32_4x4x4bf16_1k(__builtin_bit_cast(s16x4, idS), __builtin_bit_cast(s16x4, G[s][r]),
                                                                    f32x4{0.f, 0.f, 0.f, 0.f}, 0, 0, 0);
            const u32 lowb = __builtin_amdgcn_perm(v30, RP[s][t][3], 0x07000c0cu | ((u32)k << 16));
            const float cs = __uint_as_float(andor((u32)((int)Pk >> 3), vmf, lowb));
            const u32 x0 = __float_as_uint(__builtin_fmaf(ms.x, cs, MAG));
            const u32 x1 = __float_as_uint(__builtin_fmaf(ms.y, cs, MAG));
            const u32 x2 = __float_as_uint(__builtin_fmaf(ms.z, cs, MAG));
            const u32 x3 = __float_as_uint(__builtin_fmaf(ms.w, cs, MAG));
            const u32x2 pa = {__builtin_amdgcn_perm(x1, x0, 0x05040100u), __builtin_amdgcn_perm(x3, x2, 0x05040100u)};
            const u64 a = __builtin_bit_cast(u64, pa) - 0x8000800080008000ull;
            const u32 ad = __builtin_amdgcn_bitop3_b32(Pk >> 10, vma, vma, 0x08);
            OLD[s][r] = __hip_atomic_fetch_add(LP(u64, ad + P2_ACC), a, __ATOMIC_RELAXED, __HIP_MEMORY_SCOPE_WORKGROUP);
            SCHED();
        };
        auto check = [&](int s, int r) {
            const u32x2 y = __builtin_bit_cast(u32x2, OLD[s][r]);
            ovf = or3(y.x, y.y, ovf);
            PIN(ovf);
        };
        rload(0, 0); rload(0, 1); rload(0, 2);
#pragma unroll
        for (int s = 2; s < NS; s++) expand(s);
#pragma unroll
        for (int s = 0; s < NS; s++) {
            SCHED();
            if (s == 0) {
                gather(0, 0); SCHED();
                rload(1, 2); rload(1, 0);
                accum(0, 0); accum(0, 1); accum(0, 2);
                gather(0, 1); SCHED();
                rload(1, 1);
                if (third(0)) gather(0, 2);
                arrive(P2_SCR + 448);
            } else {
                await(P2_SCR + 448 + (2 * s - 1) * 4);
                if (third(s)) gather(s, 2);
                gather(s, 0); gather(s, 1);
                if (s + 1 < NS) arrive(P2_SCR + 448 + 2 * s * 4);
                rload(s + 1, 1);
                check(s - 1, 0); check(s - 1, 1);
                accum(s - 1, 3); accum(s - 1, 4); accum(s - 1, 5);
                check(s - 1, 2);
            }
            if (s + 1 < NS) {
                await(P2_SCR + 448 + 2 * s * 4);
                wstore(s + 1);
                arrive(P2_SCR + 448 + (2 * s + 1) * 4);
            }
            const int wn = wsch[s + 1] - wsch[s];
            if (wn > 0) { wload(wsch[s]); SCHED(); }
            if (s > 0) { check(s - 1, 3); check(s - 1, 4); check(s - 1, 5); }
            if (third(s)) {
                accum(s, 6); accum(s, 7); accum(s, 8);
                check(s, 6); check(s, 7); check(s, 8);
                SCHED();
            }
            rload(s + 2, 2);
            if (wn > 2) { wload(wsch[s] + 1); SCHED(); }
            if (s > 0) accum(s, 0);
            if (wn > 0) { wload(wsch[s + 1] - 1); SCHED(); }
            rload(s + 2, 0);
            if (s > 0) { accum(s, 1); accum(s, 2); }
        }
        accum(NS - 1, 3); accum(NS - 1, 4); accum(NS - 1, 5);
#pragma unroll
        for (int r = 0; r < 6; r++) check(NS - 1, r);
    } else {
        handoff();
    }
    const u32 e0 = (u32)p * W + (u32)h * WQ;
    const u32 ne = min(min((u32)WQ, W - min(W, (u32)h * WQ)), (u32)E - min((u32)E, e0));
    const bool act = w * 128 < WQ;
    u32x4 em = {0, 0, 0, 0}; u32 ed = 0;
    if (act) {
        em = __builtin_amdgcn_raw_buffer_load_b128(rM, (e0 + tid * 2) * 8, 0, 0);
        ed = __builtin_amdgcn_raw_buffer_load_b32(RSRC(dist, E * 2), (e0 + tid * 2) * 2, 0, 0);
    }
    if (ovf & 0x80008000u) *LP(u32, P2_SCR + 400) = 1;
    __syncthreads();
    slow = slow || __builtin_amdgcn_readfirstlane(*LP(u32, P2_SCR + 400)) != 0;

    u32 cw[4], wsb[16], wrb[16], wdn[8], wup[8];
#pragma unroll
    for (int k = 0; k < 4; k++) cw[k] = centers[k];
#pragma unroll
    for (int k = 0; k < 16; k++) { wsb[k] = w_sbf[k]; wrb[k] = w_rbf[k]; }
#pragma unroll
    for (int k = 0; k < 8; k++) { wdn[k] = w_down[k]; wup[k] = w_up[k]; }
    const __amdgpu_buffer_rsrc_t rX = RSRC(ws + WS_PART + (size_t)p * 64 * WQ * 16, 64 * WQ * 16);
    const __amdgpu_buffer_rsrc_t rY = RSRC(ws + WS_PART16 + (size_t)p * 64 * WQ * 8, 64 * WQ * 8);
    u32x4 own = {ABIAS, ABIAS, ABIAS, ABIAS};
    f32x4 sm0 = {0, 0, 0, 0}, sm1 = {0, 0, 0, 0};
    if (!slow) {
        for (int k = 0; k < w; k++) __builtin_amdgcn_s_sleep(4);
        if (tid < WQ / 2) {
#pragma unroll
            for (int q = 1; q < NH; q++) {
                const u32 hd = (h + q) & 7;
                const u32x4 v = *LP(u32x4, P2_ACC + (hd * WQ + tid * 2) * 8);
                __builtin_amdgcn_raw_buffer_store_b128(v, rY, ((hd * 8 + h) * WQ + tid * 2) * 8, 0, AUX_SC1);
            }
            own = *LP(u32x4, P2_ACC + (h * WQ + tid * 2) * 8);
        }
    } else {
        const u16* m2 = (const u16*)m16;
        for (int cmp = 0; cmp < 4; cmp++) {
            __syncthreads();
            for (u32 k = tid; k < MAXW / 2; k += NT) *LP(u32x4, P2_ACC + k * 16) = z4;
            __syncthreads();
            for (int r = w; r < NSUB * NS; r += 16) {
                const int ck = r / NS, s = r - ck * NS;
                const u32 e0x = __builtin_amdgcn_raw_buffer_load_b32(rT, ck * 8192 + task * 32, 0, AUX_SC1);
                const u32x4 oe = __builtin_amdgcn_raw_buffer_load_b128(rT, ck * 8192 + task * 32 + (s >> 2) * 16, 0, AUX_SC1);
                const int k4 = s & 3;
                u32 b0 = (oe.x & 0x3fffu) + (k4 > 0 ? (oe.x >> 14) & 1023u : 0) + (k4 > 1 ? oe.z & 1023u : 0) + (k4 > 2 ? (oe.z >> 10) & 1023u : 0);
                u32 b1 = k4 == 0 ? (oe.x >> 24) & 31u : k4 == 1 ? (oe.z >> 20) & 31u : k4 == 2 ? (oe.z >> 25) & 31u : (oe.x >> 29) | ((oe.z >> 30) << 3);
                if (((e0x >> 24) & 31u) == 31u) {
                    const u32 x = __builtin_amdgcn_raw_buffer_load_b32(rT, (int)(WS_OFFX - WS_OFFS) + ck * 8192 + task * 32 + s * 4, 0, AUX_SC1);
                    b0 = x & 0xffffu; b1 = x >> 16;
                }
                b0 += (u32)ck * UPC; b1 += b0;
                const u32 inb = (u32)(s * NH + h) * WI;
                for (u32 j = b0 + lane; j < b1; j += 64) {
                    const u32x4 un = __builtin_amdgcn_raw_buffer_load_b128(rR, j * 16, 0, AUX_SC1);
#pragma unroll
                    for (int k = 0; k < 3; k++) {
                        const u32 Pk = un[k];
                        if (Pk == 0) continue;
                        u32 F = (((u32)((int)Pk >> 3)) & 0x8f000000u) | 0x30000000u | (((un[3] >> (8 * k)) & 0xffu) << 16);
                        if ((F & 0x7fffffffu) == 0x30000000u) F &= 0x80000000u;
                        if ((F & 0x7fffffffu) > 0x3f800000u) F = 0x7fc00000u;
                        const float mm = bf2f(m2[(size_t)(inb + (Pk & 0x1fffu)) * 4 + cmp]);
                        __hip_atomic_fetch_add(LP(double, P2_ACC + (~(Pk >> 10) & 0x1fff8u)), (double)__uint_as_float(F) * (double)mm,
                                               __ATOMIC_RELAXED, __HIP_MEMORY_SCOPE_WORKGROUP);
                    }
                }
            }
            __syncthreads();
            for (u32 e = tid; e < MAXW; e += NT) {
                const u32 hd = e / WQ, el = e - hd * WQ;
                __builtin_amdgcn_raw_buffer_store_b32(__float_as_uint((float)*LP(double, P2_ACC + e * 8)), rX, ((hd * 8 + h) * WQ + el) * 16 + cmp * 4, 0, AUX_SC1);
            }
            if (tid < WQ / 2) {
                const float a = (float)*LP(double, P2_ACC + (h * WQ + tid * 2) * 8), b = (float)*LP(double, P2_ACC + (h * WQ + tid * 2) * 8 + 8);
                sm0 = f32x4{cmp == 0 ? a : sm0.x, cmp == 1 ? a : sm0.y, cmp == 2 ? a : sm0.z, cmp == 3 ? a : sm0.w};
                sm1 = f32x4{cmp == 0 ? b : sm1.x, cmp == 1 ? b : sm1.y, cmp == 2 ? b : sm1.z, cmp == 3 ? b : sm1.w};
            }
        }
        asm volatile("s_waitcnt vmcnt(0)" ::: "memory");
        __syncthreads();
    }
    if (act) {
        SCHED();
        f32x2 gs[4], hs[4];
        auto gates = [&](int j0, int j1) {
            const f32x2 d = {half_of(ed, 0), half_of(ed, 1)};
#pragma unroll
            for (int j = j0; j < j1; j++) {
                const f32x2 df = rbf16x2(d - half_of(cw[j >> 1], j));
                const f32x2 sq2 = rbf16x2(df * df);
                const f32x2 ar = rbf16x2(sq2 * -10.0f) * 1.4426950408889634f;
                const f32x2 rb = rbf16x2(f32x2{__builtin_amdgcn_exp2f(ar.x), __builtin_amdgcn_exp2f(ar.y)});
#pragma unroll
                for (int k = 0; k < 4; k++) {
                    gs[k] += rb * half_of(wsb[j * 2 + (k >> 1)], k);
                    hs[k] += rb * half_of(wrb[j * 2 + (k >> 1)], k);
                }
            }
        };
#pragma unroll
        for (int k = 0; k < 4; k++) { gs[k] = f32x2{0.f, 0.f}; hs[k] = f32x2{0.f, 0.f}; }
        gates(0, 3);
#pragma unroll
        for (int k = 0; k < 4; k++) { PIN(gs[k]); PIN(hs[k]); }
        asm volatile("s_waitcnt vmcnt(0)" ::: "memory");
        gu32* flags = (gu32*)(ws + WS_FLAG) + (p * 16 + w) * 32;
        if (lane == 0) __hip_atomic_store(flags + h, tag + (slow ? 1u : 0u), __ATOMIC_RELAXED, __HIP_MEMORY_SCOPE_AGENT);
        gates(3, 8);
#pragma unroll
        for (int k = 0; k < 4; k++) { hs[k] = rbf16x2(hs[k]); PIN(gs[k]); PIN(hs[k]); }
#pragma unroll
        for (int k = 0; k < 8; k++) asm volatile("" : "+s"(wdn[k]), "+s"(wup[k]));
        u32 modes;
        {
            gu32* f = flags + ((h + 1 + min(lane, 6)) & 7);
            u32 v = 0;
#pragma unroll 1
            for (u32 spins = 0; spins < (1u << 22); spins++) {
                v = __hip_atomic_load(f, __ATOMIC_RELAXED, __HIP_MEMORY_SCOPE_AGENT) - tag;
                if (__all(v <= 1u)) break;
                __builtin_amdgcn_s_sleep(10);
            }
            modes = (u32)__ballot(v == 1u) & 0x7fu;
            asm volatile("" ::: "memory");
        }
        if (tid * 2 < ne) {
            auto unb = [&](u32x4 v) -> u32x4 {
                const u32x2 a = __builtin_bit_cast(u32x2, __builtin_bit_cast(u64, u32x2{v.x, v.y}) + 0x2000200020002000ull);
                const u32x2 b = __builtin_bit_cast(u32x2, __builtin_bit_cast(u64, u32x2{v.z, v.w}) + 0x2000200020002000ull);
                return u32x4{a.x, a.y, b.x, b.y};
            };
            own = unb(own);
            u32x4 lo = own & 0xffffu, hi = own >> 16;
            if (modes == 0) {
                u32x4 x[NH - 1];
#pragma unroll
                for (int q = 1; q < NH; q++)
                    x[q - 1] = __builtin_amdgcn_raw_buffer_load_b128(rY, ((h * 8 + ((h + q) & 7)) * WQ + tid * 2) * 8, 0, AUX_SC1);
#pragma unroll
                for (int q = 0; q < NH - 1; q++) { const u32x4 y = unb(x[q]); lo += y & 0xffffu; hi += y >> 16; }
            } else {
                for (int q = 1; q < NH; q++) {
                    const u32 idx = (h * 8 + ((h + q) & 7)) * WQ + tid * 2;
                    u32x4 x = {ABIAS, ABIAS, ABIAS, ABIAS};
                    if ((modes >> (q - 1)) & 1) {
                        sm0 += __builtin_bit_cast(f32x4, __builtin_amdgcn_raw_buffer_load_b128(rX, idx * 16, 0, AUX_SC1));
                        sm1 += __builtin_bit_cast(f32x4, __builtin_amdgcn_raw_buffer_load_b128(rX, idx * 16 + 16, 0, AUX_SC1));
                    } else x = __builtin_amdgcn_raw_buffer_load_b128(rY, idx * 8, 0, AUX_SC1);
                    x = unb(x);
                    lo += x & 0xffffu; hi += x >> 16;
                }
            }
            lo -= NH * (AB16 + 0x2000u); hi -= NH * (AB16 + 0x2000u);
            if (modes != 0x7f || !slow) {
                sm0 += f32x4{(float)(int)lo.x, (float)(int)hi.x, (float)(int)lo.y, (float)(int)hi.y} * quantum;
                sm1 += f32x4{(float)(int)lo.z, (float)(int)hi.z, (float)(int)lo.w, (float)(int)hi.w} * quantum;
            }
            f32x2 agg[4], o[4];
#pragma unroll
            for (int k = 0; k < 4; k++) {
                f32x2 v = {0.f, 0.f};
#pragma unroll
                for (int j = 0; j < 4; j++) v += f32x2{sm0[j], sm1[j]} * half_of(wdn[j * 2 + (k >> 1)], k);
                agg[k] = rbf16x2(v * gs[k]);
            }
#pragma unroll
            for (int k = 0; k < 4; k++) {
                f32x2 v = {0.f, 0.f};
#pragma unroll
                for (int j = 0; j < 4; j++) v += agg[j] * half_of(wup[j * 2 + (k >> 1)], k);
                o[k] = f32x2{half_of(em[k >> 1], k), half_of(em[2 + (k >> 1)], k)} + rbf16x2(rbf16x2(v) * hs[k]);
            }
            const u32x4 ov = {pk_bf16(o[0].x, o[1].x), pk_bf16(o[2].x, o[3].x), pk_bf16(o[0].y, o[1].y), pk_bf16(o[2].y, o[3].y)};
            __builtin_amdgcn_raw_buffer_store_b128(ov, RSRC(out, E * 8), (e0 + tid * 2) * 8, 0, AUX_SC1 | 1);
        }
    }
    asm volatile(".Lke%c0:" :: "i"((int)FULL));
}

__global__ void __launch_bounds__(256)
gemnet_scatter_kernel(const u16* __restrict__ m, const int* __restrict__ idx_in, const int* __restrict__ idx_out,
                      const u16* __restrict__ angle, const u16* __restrict__ ang_freq, float* acc, int T, int De, int L)
{
    const long long g = (long long)blockIdx.x * 256 + threadIdx.x;
    const int t = (int)(g / De), k = (int)(g - (long long)t * De);
    if (t >= T) return;
    const float mv = bf2f(m[(size_t)idx_in[t] * De + k]), a = bf2f(angle[t]);
    for (int l = 0; l < L; l++)
        atomicAdd(&acc[((size_t)idx_out[t] * L + l) * De + k], rbf16(cosf(rbf16(bf2f(ang_freq[l]) * a))) * mv);
}
__global__ void __launch_bounds__(256)
gemnet_edge_kernel(const u16* __restrict__ m, const u16* __restrict__ dist, const u16* __restrict__ centers,
                   const u16* __restrict__ w_rbf, const u16* __restrict__ w_sbf, const u16* __restrict__ w_down,
                   const u16* __restrict__ w_up, const float* __restrict__ acc, u16* __restrict__ out, int E, int De, int NR, int L)
{
    const long long g = (long long)blockIdx.x * 256 + threadIdx.x;
    const int e = (int)(g / De), k = (int)(g - (long long)e * De);
    if (e >= E) return;
    const float d = bf2f(dist[e]);
    auto rbf = [&](int r) { const float df = rbf16(d - bf2f(centers[r])); return rbf16(expf(rbf16(-10.0f * rbf16(df * df)))); };
    float hk = 0.f, up = 0.f;
    for (int r = 0; r < NR; r++) hk += rbf(r) * bf2f(w_rbf[r * De + k]);
    for (int j = 0; j < De; j++) {
        float agg = 0.f;
        for (int l = 0; l < L; l++) {
            float gl = 0.f, v = 0.f;
            for (int r = 0; r < NR; r++) gl += rbf(r) * bf2f(w_sbf[(r * L + l) * De + j]);
            for (int i = 0; i < De; i++) v += acc[((size_t)e * L + l) * De + i] * bf2f(w_down[i * De + j]);
            agg += v * gl;
        }
        up += rbf16(agg) * bf2f(w_up[j * De + k]);
    }
    const float o = bf2f(m[(size_t)e * De + k]) + rbf16(rbf16(up) * rbf16(hk));
    out[(size_t)e * De + k] = (u16)pk_bf16(o, o);
}

extern "C" void kernel_launch(void* const* d_in, const int* in_sizes, int n_in,
                              void* d_out, int out_size, void* d_ws, size_t ws_size,
                              hipStream_t stream) {
    const int E = in_sizes[3], T = in_sizes[1], NR = in_sizes[5], L = in_sizes[6];
    const int De = in_sizes[0] / E;
    const u32 W = (((u32)E + 31) / 32 + 15) & ~15u;
    const u32 WI = (((u32)E + NH * NS - 1) / (NH * NS) + 15) & ~15u;
    const u64 magW = (0x100000000ull + W - 1) / W, magI = (0x100000000ull + WI - 1) / WI;
    const int T4 = T / 4;
    const int RF = T4 / (NWG * NT);
    const int RQ = (T4 - RF * NWG * NT + NWG - 1) / NWG;
    int dev = 0, cus = 0;
    (void)hipGetDevice(&dev);
    (void)hipDeviceGetAttribute(&cus, hipDeviceAttributeMultiprocessorCount, dev);
    const bool fused = De == 4 && NR == 8 && L == 1 && E % 2 == 0 && T % 4 == 0 && RF < NQ && W <= (u32)MAXW && WI <= (u32)MAXWI &&
                       magW < (1u << 24) && magI < (1u << 24) &&
                       (u64)E * (magW * W - 0x100000000ull) < 0x100000000ull && (u64)E * (magI * WI - 0x100000000ull) < 0x100000000ull &&
                       ws_size >= WS_END && cus >= NWG;
    if (fused) {
        const float rfac = 1.2f * (7.0f + 4.5f * sqrtf((float)T / ((float)E * NH))) / (float)E;
        auto kf = RF == NQ - 1 ? gemnet_fused_kernel<true> : gemnet_fused_kernel<false>;
        (void)hipFuncSetAttribute((const void*)kf, hipFuncAttributeMaxDynamicSharedMemorySize, LDS_BYTES);
        kf<<<NWG, NT, LDS_BYTES, stream>>>((const u32x4*)d_in[1], (const u32x4*)d_in[2], (const u32x2*)d_in[4],
            (const u16*)d_in[6], (const u32x4*)d_in[0], (char*)d_ws, (const u16*)d_in[3],
            (const u32*)d_in[5], (const u32*)d_in[7], (const u32*)d_in[8], (const u32*)d_in[9], (const u32*)d_in[10],
            (u32x2*)d_out, T4, RF, RQ, E, W, (u32)magW, WI, (u32)magI, rfac);
    } else {
        (void)hipMemsetAsync(d_ws, 0, (size_t)E * L * De * 4, stream);
        gemnet_scatter_kernel<<<(unsigned)(((long long)T * De + 255) / 256), 256, 0, stream>>>((const u16*)d_in[0], (const int*)d_in[1],
            (const int*)d_in[2], (const u16*)d_in[4], (const u16*)d_in[6], (float*)d_ws, T, De, L);
        gemnet_edge_kernel<<<(unsigned)(((long long)E * De + 255) / 256), 256, 0, stream>>>((const u16*)d_in[0], (const u16*)d_in[3],
            (const u16*)d_in[5], (const u16*)d_in[7], (const u16*)d_in[8], (const u16*)d_in[9], (const u16*)d_in[10],
            (const float*)d_ws, (u16*)d_out, E, De, NR, L);
    }
}
```

```cpp
#include <hip/hip_runtime.h>
#include <hip/hip_bf16.h>
#include <stdint.h>
#include <math.h>

typedef unsigned short u16;
typedef unsigned int u32;
typedef unsigned long long u64;
typedef u32 u32x4 __attribute__((ext_vector_type(4)));
typedef u32 u32x2 __attribute__((ext_vector_type(2)));
typedef float f32x4 __attribute__((ext_vector_type(4)));
typedef float f32x2 __attribute__((ext_vector_type(2)));
typedef __bf16 bf16x2 __attribute__((ext_vector_type(2)));
typedef short s16x4 __attribute__((ext_vector_type(4)));
typedef __attribute__((address_space(1))) u32 gu32;

#pragma clang diagnostic ignored "-Wint-to-pointer-cast"
#define PIN(x) asm volatile("" : "+v"(x))
#define LP(T, a) ((__attribute__((address_space(3))) T*)(u32)(a))
#define SCHED() __builtin_amdgcn_sched_barrier(0)
#define RSRC(p, bytes) __builtin_amdgcn_make_buffer_rsrc((void*)(p), 0, (bytes), 0x00020000)
#define AUX_NT 2
#define AUX_SC1 16

constexpr int NT = 1024;
constexpr int NWG = 256;
constexpr int NS = 7;
constexpr int NH = 8;
constexpr int NBKT = NWG * NS;
constexpr int NQ = 13;
constexpr int WQ = 1568;
constexpr int MAXW = NH * WQ;
constexpr int MAXWI = 7168;
constexpr int NSUB = 2 * NWG;
constexpr int MAXN = 7 * 4096;
constexpr int UMAX = (MAXN + 2 * NBKT) / 3 + 1;
constexpr int UPC = 10760;
constexpr int LDS_BYTES = 163840;
constexpr int P1_DUMMY = NBKT * 4;
constexpr int P1_SCR = P1_DUMMY + 256;
constexpr int P1_STG = P1_SCR + 256;
constexpr int P1_UA = (LDS_BYTES - P1_STG) / 16;
constexpr int ND = (P1_UA + NT - 1) / NT;
static_assert(UMAX < UPC && 3 * UMAX < 65535 && P1_UA % 32 == 0 && ND == 10, "geometry");
constexpr int P2_ACC = LDS_BYTES - MAXW * 8;
constexpr int LCAP = 3 * NT;
constexpr int P2_HEAD = LDS_BYTES - NS * LCAP * 4;
constexpr int P2_SCR = MAXWI * 8;
constexpr u32 AB16 = 0x4000u;
constexpr u32 ABIAS = AB16 * 0x10001u;
static_assert(P2_SCR == NT * 56 && P2_SCR + 1024 <= P2_ACC && P2_ACC <= P2_HEAD && P2_ACC < 65536 && P2_ACC + 0x1fff8 >= LDS_BYTES, "layout");

constexpr size_t WS_REC = 0;
constexpr size_t WS_OFFS = WS_REC + (size_t)NSUB * UPC * 16;
constexpr size_t WS_OFFX = WS_OFFS + (size_t)NSUB * 256 * 32;
constexpr size_t WS_STAT = WS_OFFX + (size_t)NSUB * 256 * 32;
constexpr size_t WS_EPOCH = WS_STAT + 4096;
constexpr size_t WS_PHASE = WS_EPOCH + 4096;
constexpr size_t WS_FLAG = WS_PHASE + 8 * 1024;
constexpr size_t WS_PART = WS_FLAG + 32 * 16 * 128;
constexpr size_t WS_PART16 = WS_PART + (size_t)32 * 64 * WQ * 16;
constexpr size_t WS_END = WS_PART16 + (size_t)32 * 64 * WQ * 8;

__device__ __forceinline__ float bf2f(u16 v) { return __uint_as_float(((u32)v) << 16); }
__device__ __forceinline__ u32 pk_bf16(float a, float b) {
    f32x2 v = {a, b};
    bf16x2 r = __builtin_convertvector(v, bf16x2);
    return __builtin_bit_cast(u32, r);
}
__device__ __forceinline__ float rbf16(float v) { return __uint_as_float(pk_bf16(v, v) << 16); }
__device__ __forceinline__ f32x2 rbf16x2(f32x2 v) {
    const u32 t = pk_bf16(v.x, v.y);
    return f32x2{__uint_as_float(t << 16), __uint_as_float(t & 0xffff0000u)};
}
template <int CTRL, int RM>
__device__ __forceinline__ u32 dpp0(u32 v) { return __builtin_amdgcn_update_dpp(0u, v, CTRL, RM, 0xf, false); }
__device__ __forceinline__ u32 row_incl_sum(u32 v) {
    v += dpp0<0x111, 0xf>(v);
    v += dpp0<0x112, 0xf>(v);
    v += dpp0<0x114, 0xf>(v);
    v += dpp0<0x118, 0xf>(v);
    return v;
}
__device__ __forceinline__ u32 half_incl_sum(u32 v) {
    v = row_incl_sum(v);
    return v + dpp0<0x142, 0xa>(v);
}
__device__ __forceinline__ float wave_incl_sumf(float v) {
    v += __uint_as_float(dpp0<0x111, 0xf>(__float_as_uint(v)));
    v += __uint_as_float(dpp0<0x112, 0xf>(__float_as_uint(v)));
    v += __uint_as_float(dpp0<0x114, 0xf>(__float_as_uint(v)));
    v += __uint_as_float(dpp0<0x118, 0xf>(__float_as_uint(v)));
    v += __uint_as_float(dpp0<0x142, 0xa>(__float_as_uint(v)));
    v += __uint_as_float(dpp0<0x143, 0xc>(__float_as_uint(v)));
    return v;
}
__device__ __forceinline__ u32 wave_incl_max(u32 v) {
    v = max(v, dpp0<0x111, 0xf>(v));
    v = max(v, dpp0<0x112, 0xf>(v));
    v = max(v, dpp0<0x114, 0xf>(v));
    v = max(v, dpp0<0x118, 0xf>(v));
    v = max(v, dpp0<0x142, 0xa>(v));
    v = max(v, dpp0<0x143, 0xc>(v));
    return v;
}
__device__ __forceinline__ u32 mulhi24(u32 v, u32 s) { u32 r; asm("v_mul_hi_u32_u24 %0, %1, %2" : "=v"(r) : "s"(s), "v"(v)); return r; }
__device__ __forceinline__ u32 mad24(u32 a, int s, u32 c) { u32 r; asm("v_mad_i32_i24 %0, %1, %2, %3" : "=v"(r) : "v"(a), "s"(s), "v"(c)); return r; }
__device__ __forceinline__ u32 bsel(u32 m, u32 a, u32 b) { return __builtin_amdgcn_bitop3_b32(m, a, b, 0xca); }
__device__ __forceinline__ u32 andor(u32 a, u32 m, u32 b) { return __builtin_amdgcn_bitop3_b32(a, m, b, 0xea); }
__device__ __forceinline__ u32 or3(u32 a, u32 b, u32 c) { return __builtin_amdgcn_bitop3_b32(a, b, c, 0xfe); }
__device__ __forceinline__ u32 shl3_16(u32 v) { u32 r; asm("v_lshlrev_b16 %0, 3, %1" : "=v"(r) : "v"(v)); return r; }
__device__ __forceinline__ u32 shl2_16(u32 v) { u32 r; asm("v_lshlrev_b16 %0, 2, %1" : "=v"(r) : "v"(v)); return r; }
__device__ __forceinline__ u32 div3(u32 v) { return (u32)__umul24(v, 43691u) >> 17; }
__device__ __forceinline__ u32 lds_add_rtn(u32 addr, u32 v) { return __hip_atomic_fetch_add(LP(u32, addr), v, __ATOMIC_RELAXED, __HIP_MEMORY_SCOPE_WORKGROUP); }
__device__ __forceinline__ float half_of(u32 w, int k) { return __uint_as_float((k & 1) ? (w & 0xffff0000u) : (w << 16)); }

__device__ __forceinline__ float rev_large(float x) {
    const u32 b = __float_as_uint(x) & 0x7fffffffu;
    const u32 k = min((b >> 23) - 134u, 127u), j = k >> 5;
    const u32 M = ((b >> 16) & 0x7fu) | 0x80u;
    const u32 hi = j == 0 ? 0x28be60dbu : j == 1 ? 0x9391054au : j == 2 ? 0x7f09d5f4u : 0x7d4d3770u;
    const u32 lo = j == 0 ? 0x9391054au : j == 1 ? 0x7f09d5f4u : j == 2 ? 0x7d4d3770u : 0x36d8a566u;
    const u32 f = (u32)(((((u64)hi << 32) | lo) << (k & 31u)) >> 32);
    const float r = (float)(M * f) * 2.3283064365386963e-10f;
    return b >= 0x7f800000u ? __uint_as_float(0x7fc00000u) : r;
}

template <bool FULL>
__global__ void __launch_bounds__(NT, 4)
gemnet_fused_kernel(const u32x4* __restrict__ idx_in, const u32x4* __restrict__ idx_out, const u32x2* __restrict__ angle,
                    const u16* __restrict__ ang_freq, const u32x4* __restrict__ m16, char* ws,
                    const u16* __restrict__ dist, const u32* __restrict__ centers, const u32* __restrict__ w_rbf,
                    const u32* __restrict__ w_sbf, const u32* __restrict__ w_down, const u32* __restrict__ w_up,
                    u32x2* __restrict__ out, int T4, int RF, int RQ, int E, u32 W, u32 magW, u32 WI, u32 magI, float rfac)
{
    const int tid = threadIdx.x;
    const int lane = tid & 63;
    const int w = __builtin_amdgcn_readfirstlane(tid >> 6);
    const int c = blockIdx.x;
    const int m_n16 = E >> 1;
    u32 warm = 0;
    {
        u64 kpc; u32 klen;
        asm volatile("s_getpc_b64 %0\n.Lks%c2:\n\ts_mov_b32 %1, .Lke%c2-.Lks%c2" : "=s"(kpc), "=s"(klen) : "i"((int)FULL));
        if (w == 13) warm = __builtin_amdgcn_raw_buffer_load_b32(RSRC(kpc, klen), lane < 32 ? (u32)(lane * 32 + (c >> 3)) * 128u : 0xfffffff0u, 0, 0);
        if (w == 12) {
            const u32 off = lane < 16 ? (u32)(WS_FLAG - WS_STAT) + ((c >> 3) * 16 + lane) * 128
                          : lane < 24 ? (u32)(WS_PHASE - WS_STAT) + (c & 7) * 1024 + (lane - 16) * 128 : (c >> 3) * 128;
            warm = __builtin_amdgcn_raw_buffer_load_b32(RSRC(ws + WS_STAT, (int)(WS_PART - WS_STAT)), off, 0, AUX_SC1);
        }
    }
    const __amdgpu_buffer_rsrc_t rI = RSRC(idx_in, T4 * 16), rO = RSRC(idx_out, T4 * 16), rA = RSRC(angle, T4 * 8);
    const int rem0 = RF * NWG * NT + c * RQ;
    const bool valid0 = tid < min(RQ, T4 - rem0);
    auto skip = [&](int i) -> bool { return i == 0 ? w * 64 >= RQ : (!FULL && i - 1 >= RF); };
    const int mper = (m_n16 + NWG - 1) / NWG;
    const int m0i = min(c * mper, m_n16);
    const __amdgpu_buffer_rsrc_t rT = RSRC(ws + WS_OFFS, (int)(WS_FLAG - WS_OFFS));

    u32x4 rin[NQ], rout[NQ];
    u32x2 rang[NQ];
    u32 P[NQ][4], B8[NQ], BK[NQ][2];

    const u32 t16 = tid * 16, t8 = tid * 8;
    const u32 epoch = __builtin_amdgcn_raw_buffer_load_b32(rT, (int)(WS_EPOCH - WS_OFFS), 0, AUX_SC1);
    const u32x4 mv = __builtin_amdgcn_raw_buffer_load_b128(RSRC(m16 + m0i, min(mper, m_n16 - m0i) * 16), t16, 0, 0);
    auto issue = [&](int i) {
        if (i == 0) {
            const u32 q = valid0 ? rem0 + tid : 0x0fffffffu;
            rin[i] = __builtin_amdgcn_raw_buffer_load_b128(rI, q * 16, 0, AUX_NT);
            rout[i] = __builtin_amdgcn_raw_buffer_load_b128(rO, q * 16, 0, AUX_NT);
            rang[i] = __builtin_amdgcn_raw_buffer_load_b64(rA, q * 8, 0, AUX_NT);
        } else if (!skip(i)) {
            const u32 qb = ((i - 1) * NWG + c) * NT;
            rin[i] = __builtin_amdgcn_raw_buffer_load_b128(rI, t16, qb * 16, AUX_NT);
            rout[i] = __builtin_amdgcn_raw_buffer_load_b128(rO, t16, qb * 16, AUX_NT);
            rang[i] = __builtin_amdgcn_raw_buffer_load_b64(rA, t8, qb * 8, AUX_NT);
        }
    };
    issue(0); issue(1);
    const float freq = bf2f(ang_freq[0]);
    SCHED();
    const u32x4 z4 = {0, 0, 0, 0};
#pragma unroll
    for (int k = 0; k < LDS_BYTES / 16 / NT; k++) *LP(u32x4, t16 + k * NT * 16) = z4;
    __syncthreads();
    asm volatile("" :: "v"(warm));
    const u32 tag = __builtin_amdgcn_readfirstlane(epoch) + 0x9e3779b9u;
    {
        u32 lo[4], hi[4], mx = 0;
#pragma unroll
        for (int e = 0; e < 4; e++) { lo[e] = (mv[e] << 16) & 0x7fffffffu; hi[e] = mv[e] & 0x7fff0000u; mx = max(mx, max(lo[e], hi[e])); }
        mx = wave_incl_max(mx);
        f32x4 sa = {__uint_as_float(lo[0]) + __uint_as_float(lo[2]), __uint_as_float(hi[0]) + __uint_as_float(hi[2]),
                    __uint_as_float(lo[1]) + __uint_as_float(lo[3]), __uint_as_float(hi[1]) + __uint_as_float(hi[3])};
#pragma unroll
        for (int e = 0; e < 4; e++) sa[e] = wave_incl_sumf(sa[e]);
        if (lane == 63) {
            __hip_atomic_fetch_max(LP(u32, P1_SCR + 64), mx, __ATOMIC_RELAXED, __HIP_MEMORY_SCOPE_WORKGROUP);
            *LP(float, P1_SCR + 128 + w * 4) = fmaxf(fmaxf(sa[0], sa[1]), fmaxf(sa[2], sa[3]));
            u32 nm = 0;
#pragma unroll
            for (int e = 0; e < 4; e++) nm = max(nm, sa[e] > 0.f ? ~__float_as_uint(sa[e]) >> 16 : 0u);
            if (tid < min(mper, m_n16 - m0i)) __hip_atomic_fetch_max(LP(u32, P1_SCR + 68), nm, __ATOMIC_RELAXED, __HIP_MEMORY_SCOPE_WORKGROUP);
        }
    }
    const bool unit_freq = __builtin_amdgcn_readfirstlane(__float_as_uint(freq)) == 0x3f800000u;
    u32 nanacc = 0;
    u32 vm8 = 0x80008000u, vmt = 0xf8000000u, v12 = 12;
    PIN(vm8); PIN(vmt); PIN(v12);

    auto encode = [&](int i) {
        if (skip(i)) return;
        u32 x01 = rang[i].x, x23 = rang[i].y;
        if (!unit_freq) {
            x01 = pk_bf16(half_of(x01, 0) * freq, half_of(x01, 1) * freq);
            x23 = pk_bf16(half_of(x23, 0) * freq, half_of(x23, 1) * freq);
        }
        const float x[4] = {half_of(x01, 0), half_of(x01, 1), half_of(x23, 0), half_of(x23, 1)};
        float r[4];
#pragma unroll
        for (int k = 0; k < 4; k++) r[k] = x[k] * 0.15915494309189535f;
        const float rm = fmaxf(fmaxf(fabsf(r[0]), fabsf(r[1])), fmaxf(fabsf(r[2]), fabsf(r[3])));
        if (__builtin_expect(__any(rm >= 20.0f), 0)) {
#pragma unroll
            for (int k = 0; k < 4; k++) r[k] = fabsf(x[k]) >= 128.0f ? rev_large(x[k]) : r[k];
        }
        const u32 c01 = pk_bf16(__builtin_amdgcn_cosf(r[0]), __builtin_amdgcn_cosf(r[1]));
        const u32 c23 = pk_bf16(__builtin_amdgcn_cosf(r[2]), __builtin_amdgcn_cosf(r[3]));
        nanacc = or3(c01, c23, nanacc);
        PIN(nanacc);
        B8[i] = __builtin_amdgcn_perm(c23, c01, 0x06040200u);
        const u32 t01 = bsel(vm8, c01, c01 << 3), t23 = bsel(vm8, c23, c23 << 3);
        const u32 F[4] = {t01 << 16, t01, t23 << 16, t23};
        u32 bk[4];
#pragma unroll
        for (int k = 0; k < 4; k++) {
            const u32 vi = rin[i][k], vo = rout[i][k];
            const u32 q = mulhi24(vi, magI);
            const u32 p = mulhi24(vo, magW);
            const u32 li = mad24(q, -(int)WI, vi);
            const u32 lo = mad24(p, -(int)W, vo);
            P[i][k] = bsel(vmt, F[k], ((lo << 13) + li) ^ 0x07ffe000u);
            bk[k] = shl2_16((q << 5) + p);
            if (i == 0) bk[k] = valid0 ? bk[k] : (u32)(P1_DUMMY + lane * 4);
            lds_add_rtn(bk[k], i < 7 ? 1u : 0x10000u);
        }
        BK[i][0] = bk[0] | (bk[1] << 16);
        BK[i][1] = bk[2] | (bk[3] << 16);
        PIN(BK[i][0]); PIN(BK[i][1]); PIN(B8[i]); PIN(P[i][0]); PIN(P[i][1]); PIN(P[i][2]); PIN(P[i][3]);
    };
    const __amdgpu_buffer_rsrc_t rR = RSRC(ws + WS_REC, NSUB * UPC * 16);
    auto scan = [&](int sub) -> u32 {
        u32 un[7], st[7];
        if (w < 4) {
#pragma unroll
            for (int k = 0; k < 7; k++) un[k] = div3((*LP(u32, tid * 4 + k * 1024) >> (16 * sub)) + 2);
            u32 run = 0;
#pragma unroll
            for (int j = 0; j < 4; j++) {
                const u32 v = half_incl_sum(un[2 * j] | (j < 3 ? un[2 * j + 1] << 16 : 0u));
                const u32 t0 = __builtin_amdgcn_readlane(v, 31), t1 = __builtin_amdgcn_readlane(v, 63);
                const u32 tt = lane < 32 ? t0 : t1;
                st[2 * j] = run + (v & 0xffffu) - un[2 * j];
                run += tt & 0xffffu;
                if (j < 3) { st[2 * j + 1] = run + (v >> 16) - un[2 * j + 1]; run += tt >> 16; }
            }
            if ((lane & 31) == 0) *LP(u32, P1_SCR + (tid >> 5) * 4) = run;
        }
        if (w == 8) *LP(u32, P1_DUMMY + lane * 4) = 0xffffu;
        __syncthreads();
        const u32 ht = *LP(u32, P1_SCR + (lane & 7) * 4);
        u32 hs = ht + dpp0<0x111, 0xf>(ht);
        hs += dpp0<0x112, 0xf>(hs); hs += dpp0<0x114, 0xf>(hs);
        const u32 U = __builtin_amdgcn_readlane(hs, 7);
        if (w < 4) {
            const u32 b0 = __builtin_amdgcn_readlane(hs - ht, 2 * w), b1 = __builtin_amdgcn_readlane(hs - ht, 2 * w + 1);
            const u32 base = lane < 32 ? b0 : b1;
            u32 nn[7], dd[7], sat = *LP(u32, P1_SCR + 96);
#pragma unroll
            for (int k = 0; k < 7; k++) {
                st[k] += base;
                *LP(u32, tid * 4 + k * 1024) = st[k] * 3;
                nn[k] = min(un[k], 31u);
                dd[k] = k ? min(st[k] - st[k - 1], 1023u) : 0u;
                sat |= (nn[k] == 31u) | (k != 4 && dd[k] == 1023u);
            }
            if (sat) nn[0] = 31u;
            const u32 x0 = st[0] | (dd[1] << 14) | (nn[0] << 24) | (nn[3] << 29), z0 = dd[2] | (dd[3] << 10) | (nn[1] << 20) | (nn[2] << 25) | ((nn[3] >> 3) << 30);
            const u32 x1 = st[4] | (dd[5] << 14) | (nn[4] << 24), z1 = dd[6] | (nn[5] << 20) | (nn[6] << 25);
#pragma unroll
            for (int j = 0; j < 2; j++) {
                const int src = (j * 32 + (lane >> 1)) * 4;
                const u32 a0 = __builtin_amdgcn_ds_bpermute(src, x0), a1 = __builtin_amdgcn_ds_bpermute(src, x1);
                const u32 c0 = __builtin_amdgcn_ds_bpermute(src, z0), c1 = __builtin_amdgcn_ds_bpermute(src, z1);
                const u32x4 o = {(lane & 1) ? a1 : a0, tag, (lane & 1) ? c1 : c0, tag};
                __builtin_amdgcn_raw_buffer_store_b128(o, rT, (sub * NWG + c) * 8192 + w * 2048 + j * 1024 + lane * 16, 0, AUX_SC1);
            }
            if (sat) {
#pragma unroll
                for (int k = 0; k < 7; k++)
                    __builtin_amdgcn_raw_buffer_store_b32(st[k] | (un[k] << 16), rT, (int)(WS_OFFX - WS_OFFS) + (sub * NWG + c) * 8192 + tid * 32 + k * 4, 0, AUX_SC1);
            }
        }
        if (U > (u32)P1_UA) {
            for (u32 u = P1_UA + tid; u < U; u += NT) __builtin_amdgcn_raw_buffer_store_b128(z4, rR, ((sub * NWG + c) * UPC + u) * 16, 0, AUX_SC1);
            asm volatile("s_waitcnt vmcnt(0)" ::: "memory");
        }
        __syncthreads();
        return U;
    };
    u32 pos[NQ][4];
    auto cursor = [&](int i) {
        if (skip(i)) return;
        PIN(BK[i][0]); PIN(BK[i][1]);
        const u32 inc = i > 0 || valid0 ? 1u : 0u;
        pos[i][0] = lds_add_rtn(BK[i][0] & 0xffffu, inc); pos[i][1] = lds_add_rtn(BK[i][0] >> 16, inc);
        pos[i][2] = lds_add_rtn(BK[i][1] & 0xffffu, inc); pos[i][3] = lds_add_rtn(BK[i][1] >> 16, inc);
    };
    auto scatter = [&](int i, bool big) {
        if (skip(i)) return;
#pragma unroll
        for (int k = 0; k < 4; k++) {
            const u32 ps = i < 7 ? (pos[i][k] & 0xffffu) : pos[i][k];
            const u32 t = div3(ps), zz = t >> 1;
            *LP(u32, __builtin_amdgcn_bitop3_b32((ps + t) << 2, zz, v12, 0x78) + P1_STG) = P[i][k];
            const u32 src = (k & 1) ? (B8[i] >> 8) : B8[i];
            *LP(unsigned char, __builtin_amdgcn_bitop3_b32(mad24(t, 13, ps), zz, v12, 0xf2) + P1_STG) = (unsigned char)((k & 2) ? (src >> 16) : src);
        }
        if (big) {
            const u32 g = ((i >= 7) * NWG + c) * UPC * 16;
#pragma unroll
            for (int k = 0; k < 4; k++) {
                const u32 ps = pos[i][k] & 0xffffu, t = div3(ps);
                if (t >= (u32)P1_UA && ps != 0xffffu) {
                    __builtin_amdgcn_raw_buffer_store_b32(P[i][k], rR, g + (ps + t) * 4, 0, AUX_SC1);
                    __builtin_amdgcn_raw_buffer_store_b8((unsigned char)(B8[i] >> (8 * k)), rR, g + 13 * t + ps + 12, 0, AUX_SC1);
                }
            }
        }
    };
    const u32 sw1 = (tid & 8) ? ~0u : 0u, sw2 = (tid & 16) ? ~0u : 0u;
    auto unswz = [&](u32x4 v) -> u32x4 {
        const u32x4 a = {bsel(sw1, v.y, v.x), bsel(sw1, v.x, v.y), bsel(sw1, v.w, v.z), bsel(sw1, v.z, v.w)};
        return u32x4{bsel(sw2, a.z, a.x), bsel(sw2, a.w, a.y), bsel(sw2, a.x, a.z), bsel(sw2, a.y, a.w)};
    };

    auto piece = [&](int k) -> u32 {
        u32 r;
        asm volatile("v_add_u32 %0, %1, %2" : "=v"(r) : "i"(k * NT * 16), "v"(t16));
        return r;
    };

#pragma unroll
    for (int i = 0; i < 7; i++) {
        issue(i + 2);
        SCHED();
        encode(i);
        SCHED();
    }
    __syncthreads();
    const u32 UA_ = scan(0);
    if (tid == 0) {
        float t = 0.f;
#pragma unroll
        for (int k = 0; k < 4; k++) { const f32x4 v = *LP(f32x4, P1_SCR + 128 + k * 16); t += (v.x + v.y) + (v.z + v.w); }
        __builtin_amdgcn_raw_buffer_store_b128(u32x4{*LP(u32, P1_SCR + 64) | *LP(u32, P1_SCR + 68), tag, __float_as_uint(t), tag}, rT, (int)(WS_STAT - WS_OFFS) + c * 16, 0, AUX_SC1);
    }
    {
        const bool big = UA_ > (u32)P1_UA;
        cursor(0);
#pragma unroll
        for (int i = 0; i < 7; i++) {
            if (i + 1 < 7) cursor(i + 1);
            SCHED();
            scatter(i, big);
            SCHED();
            if (i & 1) encode(7 + i / 2);
            else issue(9 + i / 2);
            SCHED();
        }
    }
    __syncthreads();
    u32x4 DR[ND];
#pragma unroll
    for (int k = 0; k < ND; k++) {
        DR[k] = *LP(u32x4, P1_STG + t16 + k * NT * 16);
        *LP(u32x4, P1_STG + t16 + k * NT * 16) = z4;
    }
    SCHED();
    const __amdgpu_buffer_rsrc_t rS0 = RSRC(ws + WS_REC + (size_t)(u32)(c * UPC) * 16u, min((UA_ + 7) & ~7u, (u32)P1_UA) * 16);
    const int h = c & 7, p = c >> 3;
    const int task = h * 32 + p;
    const bool sg = lane >= 32;
    const int cc = w * 32 + (lane & 31);
    u32x4 o4 = {0, 0, 0, 0}, mxv = {0, tag, 0, tag};
    auto load_tables = [&]() {
        o4 = __builtin_amdgcn_raw_buffer_load_b128(rT, cc * 8192 + task * 32 + (sg ? 16 : 0), 0, AUX_SC1);
        if (w < 4) mxv = __builtin_amdgcn_raw_buffer_load_b128(rT, (int)(WS_STAT - WS_OFFS) + t16, 0, AUX_SC1);
        SCHED();
    };
    encode(10); SCHED();
    encode(11); SCHED();
    encode(12); SCHED();
    if (nanacc & 0x40004000u) *LP(u32, P1_SCR + 96) = 1;
    __syncthreads();
    const u32 UB_ = scan(1);
    if (FULL && w < 8) load_tables();
    {
        const bool big = UB_ > (u32)P1_UA;
        cursor(7);
#pragma unroll
        for (int i = 7; i < NQ; i++) {
            if (i + 1 < NQ) cursor(i + 1);
            SCHED();
#pragma unroll
            for (int k = 2 * (i - 8); k < 2 * (i - 7); k++)
                if (k >= 0) { __builtin_amdgcn_raw_buffer_store_b128(unswz(DR[k]), rS0, piece(k), 0, AUX_SC1); SCHED(); }
            scatter(i, big);
            SCHED();
        }
    }
    __syncthreads();

#pragma unroll
    for (int k = 0; k < ND; k++) DR[k] = *LP(u32x4, P1_STG + t16 + k * NT * 16);
    const __amdgpu_buffer_rsrc_t rS1 = RSRC(ws + WS_REC + (size_t)(u32)((NWG + c) * UPC) * 16u, min((UB_ + 7) & ~7u, (u32)P1_UA) * 16);
    auto stores = [&](int k0, int k1) {
#pragma unroll
        for (int k = k0; k < k1; k++) { __builtin_amdgcn_raw_buffer_store_b128(unswz(DR[k]), rS1, piece(k), 0, AUX_SC1); SCHED(); }
    };
    auto arrive = [&](int ad) { asm volatile("" ::: "memory"); SCHED(); if (lane == 0) lds_add_rtn(ad, 1u); SCHED(); };
    auto await = [&](int ad) {
        SCHED();
        while (__builtin_amdgcn_readfirstlane(__hip_atomic_load(LP(u32, ad), __ATOMIC_RELAXED, __HIP_MEMORY_SCOPE_WORKGROUP)) < 16u)
            __builtin_amdgcn_s_sleep(2);
        asm volatile("" ::: "memory");
        SCHED();
    };
    if (!FULL || w >= 8) load_tables();
    stores(0, 1);
    __syncthreads();
#pragma unroll
    for (int k = 0; k < 5; k++) *LP(u32x4, P2_HEAD + t16 + k * NT * 16) = z4;
    if (w < 4) *LP(u32x4, P2_HEAD + t16 + 5 * NT * 16) = z4;
    if (tid == 0) *LP(u32, P2_SCR + 400) = 0;
    if (tid < 16) *LP(u32, P2_SCR + 448 + tid * 4) = 0;
    SCHED();
#pragma unroll 1
    for (u32 spins = 0; spins < (1u << 20) && !__all(o4.y == tag && o4.w == tag && mxv.y == tag && mxv.w == tag); spins++) {
        asm volatile("" ::: "memory");
        load_tables();
    }
    u32 n[4], inc[4], st[4];
    n[0] = (o4.x >> 24) & 31u; n[1] = (o4.z >> 20) & 31u; n[2] = (o4.z >> 25) & 31u; n[3] = (o4.x >> 29) | ((o4.z >> 30) << 3);
    st[0] = o4.x & 0x3fffu; st[1] = st[0] + ((o4.x >> 14) & 1023u); st[2] = st[1] + (o4.z & 1023u); st[3] = st[2] + ((o4.z >> 10) & 1023u);
#pragma unroll
    for (int k = 0; k < 4; k++) inc[k] = half_incl_sum(k == 0 && n[0] == 31u ? (u32)LCAP + 1 : n[k]);
    if ((lane & 31) == 31) {
#pragma unroll
        for (int k = 0; k < 4; k++) *LP(u32, P2_SCR + 512 + ((sg * 4 + k) * 16 + w) * 4) = inc[k];
    }
    if (w < 4) {
        const u32 mx = wave_incl_max(mxv.x & 0xffff0000u), nm = wave_incl_max(mxv.x & 0xffffu);
        const float sm = wave_incl_sumf(__uint_as_float(mxv.z));
        if (lane == 63) { *LP(u32, P2_SCR + 384 + w * 4) = mx; *LP(float, P2_SCR + 416 + w * 4) = sm; *LP(u32, P2_SCR + 432 + w * 4) = nm; }
    }
    arrive(P1_SCR + 72);
    stores(1, 4);
    await(P1_SCR + 72);
    u64 m3;
    bool slow;
    int lgsh;
    {
        const u32 ta = *LP(u32, P2_SCR + 512 + lane * 4), tb = *LP(u32, P2_SCR + 768 + lane * 4);
        u32 ia = row_incl_sum(ta), ib = row_incl_sum(tb);
        slow = __any(max(ia, ib) > (u32)LCAP);
        lgsh = slow ? 6 : 3;
        m3 = (__ballot(ia > (u32)(2 * NT + w * 64)) & 0x8000800080008000ull) | ((__ballot(ib > (u32)(2 * NT + w * 64)) & 0x8000800080008000ull) >> 1);
        const u32 tt = __builtin_amdgcn_readlane(w < 4 ? ia : ib, (w & 3) * 16 + 15);
        ia -= ta; ib -= tb;
        bool more = false;
        u32 nx[4], en[4], dl[4];
        const u32 hb0 = P2_HEAD + (sg ? 4 * LCAP * 4 : 0);
#pragma unroll
        for (int k = 0; k < 4; k++) {
            const u32 ca = __builtin_amdgcn_readlane(ia, k * 16 + w), cb = __builtin_amdgcn_readlane(ib, k * 16 + w);
            const u32 carry = sg ? cb : ca;
            const u32 ex = carry + inc[k] - n[k];
            en[k] = min(carry + inc[k], (u32)LCAP);
            dl[k] = ((u32)cc * UPC + st[k] - ex) * 16;
            const u32 hb = hb0 + k * LCAP * 4;
            *LP(u32, ex < en[k] ? hb + ex * 4 : LDS_BYTES) = dl[k];
            const u32 b1 = (ex | 7u) + 1;
            *LP(u32, b1 < en[k] ? hb + b1 * 4 : LDS_BYTES) = dl[k];
            nx[k] = b1 + 8;
            more |= nx[k] < en[k];
        }
        if (more) {
#pragma unroll
            for (int k = 0; k < 4; k++)
                for (u32 ps = nx[k]; ps < en[k]; ps += 8) *LP(u32, hb0 + k * LCAP * 4 + ps * 4) = dl[k];
        }
        if (w < NS) {
            for (u32 ps = (tt & ~7u) + lane * 8; ps < (u32)LCAP; ps += 512) *LP(u32, P2_HEAD + (w * LCAP + max(ps, tt)) * 4) = 0xf0000000u;
        }
    }
    const u32x4 mq = *LP(u32x4, P2_SCR + 384);
    const u32 maxbits = __builtin_amdgcn_readfirstlane(max(max(mq[0], mq[1]), max(mq[2], mq[3])));
    const f32x4 sq = *LP(f32x4, P2_SCR + 416);
    const u32x4 nq = *LP(u32x4, P2_SCR + 432);
    const float asum = (sq.x + sq.y) + (sq.z + sq.w);
    const float amin = __uint_as_float(~max(max(nq[0], nq[1]), max(nq[2], nq[3])) << 16);
    const float rtyp = rfac * asum;
    const float range = fmaxf(rtyp, 2.002f * __uint_as_float(maxbits));
    slow = slow || maxbits < 0x10000000u || maxbits > 0x70000000u || __any(range > 2.0f * rtyp || amin * (float)E < 16.0f * asum);
    arrive(P1_SCR + 76);
    stores(4, 7);
    await(P1_SCR + 76);
    u32 hv[NS][3];
#pragma unroll
    for (int s = 0; s < NS; s++)
#pragma unroll
        for (int t = 0; t < 3; t++) hv[s][t] = *LP(u32, P2_HEAD + (s * LCAP + t * NT + tid) * 4);
    stores(7, ND);
    asm volatile("s_waitcnt vmcnt(0) lgkmcnt(0)\n\ts_barrier" ::: "memory");
    if (w == 14 && lane < 8) __builtin_amdgcn_raw_buffer_store_b32(tag, rT, (int)(WS_PHASE - WS_OFFS) + lane * 1024 + c * 4, 0, AUX_SC1);
    const __amdgpu_buffer_rsrc_t rM = RSRC(m16, m_n16 * 16);
    u32x4 wr[NS][3]; u32x2 wr3[NS];
    auto wload = [&](int j) {
        const int s = j >> 2, k = j & 3;
        const u32 base = (u32)(s * NH + h) * WI * 8;
        if (k < 3) wr[s][k] = __builtin_amdgcn_raw_buffer_load_b128(rM, base + k * 16384 + t16, 0, 0);
        else wr3[s] = __builtin_amdgcn_raw_buffer_load_b64(rM, base + 49152 + t8, 0, 0);
    };
    auto wstore = [&](int s) {
#pragma unroll
        for (int k = 0; k < 3; k++) *LP(u32x4, k * 16384 + t16) = wr[s][k];
        *LP(u32x2, 49152 + t8) = wr3[s];
    };
#pragma unroll
    for (int j = 0; j < 4; j++) wload(j);
    SCHED();
    {
        const u32x4 b4 = {ABIAS, ABIAS, ABIAS, ABIAS};
#pragma unroll
        for (int k = 0; k < 6; k++) *LP(u32x4, P2_ACC + t16 + k * NT * 16) = b4;
        if (w < 2) *LP(u32x4, P2_ACC + t16 + 6 * NT * 16) = b4;
    }
    u32 p16[3] = {t16, t16 + NT * 16, t16 + 2 * NT * 16};
    PIN(p16[1]); PIN(p16[2]);
    auto expand = [&](int s) {
#pragma unroll
        for (int t = 0; t < 3; t++) {
            u32 v = hv[s][t];
            v = max(v, dpp0<0x111, 0xf>(v));
            v = max(v, dpp0<0x112, 0xf>(v));
            v = max(v, dpp0<0x114, 0xf>(v));
            hv[s][t] = v + p16[t];
        }
#pragma unroll
        for (int t = 0; t < 3; t++) PIN(hv[s][t]);
    };
    expand(0); expand(1);
    SCHED();
    wstore(0);
    asm volatile("" :: "s"(centers[0]), "s"(w_sbf[0]), "s"(w_rbf[0]), "s"(w_down[0]), "s"(w_up[0]));

    const u32 sbits = __builtin_amdgcn_readfirstlane(__float_as_uint((float)AB16 / range)) & 0xffff0000u;
    const float quantum = __uint_as_float(__builtin_amdgcn_readfirstlane(__float_as_uint(1.0f / __uint_as_float(sbits))));
    u32 ovf = 0;
    auto handoff = [&]() {
        if (w == 15) {
#pragma unroll 1
            for (u32 spins = 0; spins < (1u << 20); spins++) {
                asm volatile("" ::: "memory");
                const u32x4 v = __builtin_amdgcn_raw_buffer_load_b128(rT, (int)(WS_PHASE - WS_OFFS) + h * 1024 + lane * 16, 0, AUX_SC1);
                if (__all(v.x == tag && v.y == tag && v.z == tag && v.w == tag)) break;
                __builtin_amdgcn_s_sleep(4);
            }
        }
        asm volatile("s_waitcnt lgkmcnt(0)\n\ts_barrier" ::: "memory");
        if (c == 0 && tid == 0) __builtin_amdgcn_raw_buffer_store_b32(tag, rT, (int)(WS_EPOCH - WS_OFFS), 0, AUX_SC1);
    };
    if (!slow) {
        u32x2 idS = {(lane & 3) == 0 ? sbits >> 16 : (lane & 3) == 1 ? sbits : 0u, (lane & 3) == 2 ? sbits >> 16 : (lane & 3) == 3 ? sbits : 0u};
        float MAG = 8421376.0f;
        u32 vmf = 0x8f000000u, v30 = 0x30000000u, vma = 0x1fff8u;
        PIN(MAG); PIN(vmf); PIN(v30); PIN(idS); PIN(vma);
        auto third = [&](int s) -> bool { return (m3 >> (16 * (s & 3) + 15 - (s >> 2))) & 1; };
        u32x4 RP[NS][3];
        u32x2 G[NS][9];
        u64 OLD[NS][9];
        constexpr int wsch[NS + 1] = {16, 18, 20, 23, 25, 28, 28, 28};
#pragma unroll
        for (int j = 4; j < wsch[0]; j++) wload(j);
        SCHED();
        handoff();
        auto rload = [&](int s, int t) {
            if (s < NS && (t < 2 || third(s))) RP[s][t] = __builtin_amdgcn_raw_buffer_load_b128(rR, hv[s][t], 0, AUX_SC1);
            SCHED();
        };
        auto gather = [&](int s, int t) {
#pragma unroll
            for (int k = 0; k < 3; k++) G[s][3 * t + k] = *LP(u32x2, shl3_16(RP[s][t][k]));
        };
        auto accum = [&](int s, int r) {
            const int t = r / 3, k = r % 3;
            const u32 Pk = RP[s][t][k];
            const f32x4 ms = __builtin_amdgcn_mfma_f32_4x4x4bf16_1k(__builtin_bit_cast(s16x4, idS), __builtin_bit_cast(s16x4, G[s][r]),
                                                                    f32x4{0.f, 0.f, 0.f, 0.f}, 0, 0, 0);
            const u32 lowb = __builtin_amdgcn_perm(v30, RP[s][t][3], 0x07000c0cu | ((u32)k << 16));
            const float cs = __uint_as_float(andor((u32)((int)Pk >> 3), vmf, lowb));
            const u32 x0 = __float_as_uint(__builtin_fmaf(ms.x, cs, MAG));
            const u32 x1 = __float_as_uint(__builtin_fmaf(ms.y, cs, MAG));
            const u32 x2 = __float_as_uint(__builtin_fmaf(ms.z, cs, MAG));
            const u32 x3 = __float_as_uint(__builtin_fmaf(ms.w, cs, MAG));
            const u32x2 pa = {__builtin_amdgcn_perm(x1, x0, 0x05040100u), __builtin_amdgcn_perm(x3, x2, 0x05040100u)};
            const u64 a = __builtin_bit_cast(u64, pa) - 0x8000800080008000ull;
            const u32 ad = __builtin_amdgcn_bitop3_b32(Pk >> 10, vma, vma, 0x08);
            OLD[s][r] = __hip_atomic_fetch_add(LP(u64, ad + P2_ACC), a, __ATOMIC_RELAXED, __HIP_MEMORY_SCOPE_WORKGROUP);
            SCHED();
        };
        auto check = [&](int s, int r) {
            const u32x2 y = __builtin_bit_cast(u32x2, OLD[s][r]);
            ovf = or3(y.x, y.y, ovf);
            PIN(ovf);
        };
        rload(0, 0); rload(0, 1); rload(0, 2);
#pragma unroll
        for (int s = 2; s < NS; s++) expand(s);
#pragma unroll
        for (int s = 0; s < NS; s++) {
            SCHED();
            if (s == 0) {
                gather(0, 0); SCHED();
                rload(1, 2); rload(1, 0);
                accum(0, 0); accum(0, 1); accum(0, 2);
                gather(0, 1); SCHED();
                rload(1, 1);
                if (third(0)) gather(0, 2);
            } else {
                await(P2_SCR + 448 + s * 4);
                if (third(s)) gather(s, 2);
                gather(s, 0); gather(s, 1);
                SCHED();
                rload(s + 1, 1);
                check(s - 1, 0); check(s - 1, 1);
                accum(s - 1, 3); accum(s - 1, 4); accum(s - 1, 5);
                check(s - 1, 2);
            }
            if (s + 1 < NS) {
                asm volatile("s_waitcnt lgkmcnt(%0)\n\ts_barrier" :: "i"(s == 0 ? 0 : 3) : "memory");
                wstore(s + 1);
                arrive(P2_SCR + 448 + (s + 1) * 4);
            }
            const int wn = wsch[s + 1] - wsch[s];
            if (wn > 0) { wload(wsch[s]); SCHED(); }
            if (s > 0) { check(s - 1, 3); check(s - 1, 4); check(s - 1, 5); }
            if (third(s)) {
                accum(s, 6); accum(s, 7); accum(s, 8);
                check(s, 6); check(s, 7); check(s, 8);
                SCHED();
            }
            rload(s + 2, 2);
            if (wn > 2) { wload(wsch[s] + 1); SCHED(); }
            if (s > 0) accum(s, 0);
            if (wn > 0) { wload(wsch[s + 1] - 1); SCHED(); }
            rload(s + 2, 0);
            if (s > 0) { accum(s, 1); accum(s, 2); }
        }
        accum(NS - 1, 3); accum(NS - 1, 4); accum(NS - 1, 5);
#pragma unroll
        for (int r = 0; r < 6; r++) check(NS - 1, r);
    } else {
        handoff();
    }
    const u32 e0 = (u32)p * W + (u32)h * WQ;
    const u32 ne = min(min((u32)WQ, W - min(W, (u32)h * WQ)), (u32)E - min((u32)E, e0));
    const bool act = w * 128 < WQ;
    u32x4 em = {0, 0, 0, 0}; u32 ed = 0;
    if (act) {
        em = __builtin_amdgcn_raw_buffer_load_b128(rM, (e0 + tid * 2) * 8, 0, 0);
        ed = __builtin_amdgcn_raw_buffer_load_b32(RSRC(dist, E * 2), (e0 + tid * 2) * 2, 0, 0);
    }
    if (ovf & 0x80008000u) *LP(u32, P2_SCR + 400) = 1;
    __syncthreads();
    slow = slow || __builtin_amdgcn_readfirstlane(*LP(u32, P2_SCR + 400)) != 0;

    u32 cw[4], wsb[16], wrb[16], wdn[8], wup[8];
#pragma unroll
    for (int k = 0; k < 4; k++) cw[k] = centers[k];
#pragma unroll
    for (int k = 0; k < 16; k++) { wsb[k] = w_sbf[k]; wrb[k] = w_rbf[k]; }
#pragma unroll
    for (int k = 0; k < 8; k++) { wdn[k] = w_down[k]; wup[k] = w_up[k]; }
    const __amdgpu_buffer_rsrc_t rX = RSRC(ws + WS_PART + (size_t)p * 64 * WQ * 16, 64 * WQ * 16);
    const __amdgpu_buffer_rsrc_t rY = RSRC(ws + WS_PART16 + (size_t)p * 64 * WQ * 8, 64 * WQ * 8);
    u32x4 own = {ABIAS, ABIAS, ABIAS, ABIAS};
    f32x4 sm0 = {0, 0, 0, 0}, sm1 = {0, 0, 0, 0};
    if (!slow) {
        for (int k = 0; k < w; k++) __builtin_amdgcn_s_sleep(4);
        if (tid < WQ / 2) {
#pragma unroll
            for (int q = 1; q < NH; q++) {
                const u32 hd = (h + q) & 7;
                const u32x4 v = *LP(u32x4, P2_ACC + (hd * WQ + tid * 2) * 8);
                __builtin_amdgcn_raw_buffer_store_b128(v, rY, ((hd * 8 + h) * WQ + tid * 2) * 8, 0, AUX_SC1);
            }
            own = *LP(u32x4, P2_ACC + (h * WQ + tid * 2) * 8);
        }
    } else {
        const u16* m2 = (const u16*)m16;
        for (int cmp = 0; cmp < 4; cmp++) {
            __syncthreads();
            for (u32 k = tid; k < MAXW / 2; k += NT) *LP(u32x4, P2_ACC + k * 16) = z4;
            __syncthreads();
            for (int r = tid >> lgsh; r < NSUB * NS; r += NT >> lgsh) {
                const int ck = r / NS, s = r - ck * NS;
                const u32 e0x = __builtin_amdgcn_raw_buffer_load_b32(rT, ck * 8192 + task * 32, 0, AUX_SC1);
                const u32x4 oe = __builtin_amdgcn_raw_buffer_load_b128(rT, ck * 8192 + task * 32 + (s >> 2) * 16, 0, AUX_SC1);
                const int k4 = s & 3;
                u32 b0 = (oe.x & 0x3fffu) + (k4 > 0 ? (oe.x >> 14) & 1023u : 0) + (k4 > 1 ? oe.z & 1023u : 0) + (k4 > 2 ? (oe.z >> 10) & 1023u : 0);
                u32 b1 = k4 == 0 ? (oe.x >> 24) & 31u : k4 == 1 ? (oe.z >> 20) & 31u : k4 == 2 ? (oe.z >> 25) & 31u : (oe.x >> 29) | ((oe.z >> 30) << 3);
                if (((e0x >> 24) & 31u) == 31u) {
                    const u32 x = __builtin_amdgcn_raw_buffer_load_b32(rT, (int)(WS_OFFX - WS_OFFS) + ck * 8192 + task * 32 + s * 4, 0, AUX_SC1);
                    b0 = x & 0xffffu; b1 = x >> 16;
                }
                b0 += (u32)ck * UPC; b1 += b0;
                const u32 inb = (u32)(s * NH + h) * WI;
                for (u32 j = b0 + (lane & ((1 << lgsh) - 1)); j < b1; j += 1 << lgsh) {
                    const u32x4 un = __builtin_amdgcn_raw_buffer_load_b128(rR, j * 16, 0, AUX_SC1);
#pragma unroll
                    for (int k = 0; k < 3; k++) {
                        const u32 Pk = un[k];
                        if (Pk == 0) continue;
                        u32 F = (((u32)((int)Pk >> 3)) & 0x8f000000u) | 0x30000000u | (((un[3] >> (8 * k)) & 0xffu) << 16);
                        if ((F & 0x7fffffffu) == 0x30000000u) F &= 0x80000000u;
                        if ((F & 0x7fffffffu) > 0x3f800000u) F = 0x7fc00000u;
                        const float mm = bf2f(m2[(size_t)(inb + (Pk & 0x1fffu)) * 4 + cmp]);
                        __hip_atomic_fetch_add(LP(double, P2_ACC + (~(Pk >> 10) & 0x1fff8u)), (double)__uint_as_float(F) * (double)mm,
                                               __ATOMIC_RELAXED, __HIP_MEMORY_SCOPE_WORKGROUP);
                    }
                }
            }
            __syncthreads();
            for (u32 e = tid; e < MAXW; e += NT) {
                const u32 hd = e / WQ, el = e - hd * WQ;
                __builtin_amdgcn_raw_buffer_store_b32(__float_as_uint((float)*LP(double, P2_ACC + e * 8)), rX, ((hd * 8 + h) * WQ + el) * 16 + cmp * 4, 0, AUX_SC1);
            }
            if (tid < WQ / 2) {
                const float a = (float)*LP(double, P2_ACC + (h * WQ + tid * 2) * 8), b = (float)*LP(double, P2_ACC + (h * WQ + tid * 2) * 8 + 8);
                sm0 = f32x4{cmp == 0 ? a : sm0.x, cmp == 1 ? a : sm0.y, cmp == 2 ? a : sm0.z, cmp == 3 ? a : sm0.w};
                sm1 = f32x4{cmp == 0 ? b : sm1.x, cmp == 1 ? b : sm1.y, cmp == 2 ? b : sm1.z, cmp == 3 ? b : sm1.w};
            }
        }
        asm volatile("s_waitcnt vmcnt(0)" ::: "memory");
        __syncthreads();
    }
    if (act) {
        SCHED();
        f32x2 gs[4], hs[4];
        auto gates = [&](int j0, int j1) {
            const f32x2 d = {half_of(ed, 0), half_of(ed, 1)};
#pragma unroll
            for (int j = j0; j < j1; j++) {
                const f32x2 df = rbf16x2(d - half_of(cw[j >> 1], j));
                const f32x2 sq2 = rbf16x2(df * df);
                const f32x2 ar = rbf16x2(sq2 * -10.0f) * 1.4426950408889634f;
                const f32x2 rb = rbf16x2(f32x2{__builtin_amdgcn_exp2f(ar.x), __builtin_amdgcn_exp2f(ar.y)});
#pragma unroll
                for (int k = 0; k < 4; k++) {
                    gs[k] += rb * half_of(wsb[j * 2 + (k >> 1)], k);
                    hs[k] += rb * half_of(wrb[j * 2 + (k >> 1)], k);
                }
            }
        };
#pragma unroll
        for (int k = 0; k < 4; k++) { gs[k] = f32x2{0.f, 0.f}; hs[k] = f32x2{0.f, 0.f}; }
        gates(0, 3);
#pragma unroll
        for (int k = 0; k < 4; k++) { PIN(gs[k]); PIN(hs[k]); }
        asm volatile("s_waitcnt vmcnt(0)" ::: "memory");
        gu32* flags = (gu32*)(ws + WS_FLAG) + (p * 16 + w) * 32;
        if (lane == 0) __hip_atomic_store(flags + h, tag + (slow ? 1u : 0u), __ATOMIC_RELAXED, __HIP_MEMORY_SCOPE_AGENT);
        gates(3, 8);
#pragma unroll
        for (int k = 0; k < 4; k++) { hs[k] = rbf16x2(hs[k]); PIN(gs[k]); PIN(hs[k]); }
#pragma unroll
        for (int k = 0; k < 8; k++) asm volatile("" : "+s"(wdn[k]), "+s"(wup[k]));
        u32 modes;
        {
            gu32* f = flags + ((h + 1 + min(lane, 6)) & 7);
            u32 v = 0;
#pragma unroll 1
            for (u32 spins = 0; spins < (1u << 22); spins++) {
                v = __hip_atomic_load(f, __ATOMIC_RELAXED, __HIP_MEMORY_SCOPE_AGENT) - tag;
                if (__all(v <= 1u)) break;
                __builtin_amdgcn_s_sleep(10);
            }
            modes = (u32)__ballot(v == 1u) & 0x7fu;
            asm volatile("" ::: "memory");
        }
        if (tid * 2 < ne) {
            auto unb = [&](u32x4 v) -> u32x4 {
                const u32x2 a = __builtin_bit_cast(u32x2, __builtin_bit_cast(u64, u32x2{v.x, v.y}) + 0x2000200020002000ull);
                const u32x2 b = __builtin_bit_cast(u32x2, __builtin_bit_cast(u64, u32x2{v.z, v.w}) + 0x2000200020002000ull);
                return u32x4{a.x, a.y, b.x, b.y};
            };
            own = unb(own);
            u32x4 lo = own & 0xffffu, hi = own >> 16;
            if (modes == 0) {
                u32x4 x[NH - 1];
#pragma unroll
                for (int q = 1; q < NH; q++)
                    x[q - 1] = __builtin_amdgcn_raw_buffer_load_b128(rY, ((h * 8 + ((h + q) & 7)) * WQ + tid * 2) * 8, 0, AUX_SC1);
#pragma unroll
                for (int q = 0; q < NH - 1; q++) { const u32x4 y = unb(x[q]); lo += y & 0xffffu; hi += y >> 16; }
            } else {
                for (int q = 1; q < NH; q++) {
                    const u32 idx = (h * 8 + ((h + q) & 7)) * WQ + tid * 2;
                    u32x4 x = {ABIAS, ABIAS, ABIAS, ABIAS};
                    if ((modes >> (q - 1)) & 1) {
                        sm0 += __builtin_bit_cast(f32x4, __builtin_amdgcn_raw_buffer_load_b128(rX, idx * 16, 0, AUX_SC1));
                        sm1 += __builtin_bit_cast(f32x4, __builtin_amdgcn_raw_buffer_load_b128(rX, idx * 16 + 16, 0, AUX_SC1));
                    } else x = __builtin_amdgcn_raw_buffer_load_b128(rY, idx * 8, 0, AUX_SC1);
                    x = unb(x);
                    lo += x & 0xffffu; hi += x >> 16;
                }
            }
            lo -= NH * (AB16 + 0x2000u); hi -= NH * (AB16 + 0x2000u);
            if (modes != 0x7f || !slow) {
                sm0 += f32x4{(float)(int)lo.x, (float)(int)hi.x, (float)(int)lo.y, (float)(int)hi.y} * quantum;
                sm1 += f32x4{(float)(int)lo.z, (float)(int)hi.z, (float)(int)lo.w, (float)(int)hi.w} * quantum;
            }
            f32x2 agg[4], o[4];
#pragma unroll
            for (int k = 0; k < 4; k++) {
                f32x2 v = {0.f, 0.f};
#pragma unroll
                for (int j = 0; j < 4; j++) v += f32x2{sm0[j], sm1[j]} * half_of(wdn[j * 2 + (k >> 1)], k);
                agg[k] = rbf16x2(v * gs[k]);
            }
#pragma unroll
            for (int k = 0; k < 4; k++) {
                f32x2 v = {0.f, 0.f};
#pragma unroll
                for (int j = 0; j < 4; j++) v += agg[j] * half_of(wup[j * 2 + (k >> 1)], k);
                o[k] = f32x2{half_of(em[k >> 1], k), half_of(em[2 + (k >> 1)], k)} + rbf16x2(rbf16x2(v) * hs[k]);
            }
            const u32x4 ov = {pk_bf16(o[0].x, o[1].x), pk_bf16(o[2].x, o[3].x), pk_bf16(o[0].y, o[1].y), pk_bf16(o[2].y, o[3].y)};
            __builtin_amdgcn_raw_buffer_store_b128(ov, RSRC(out, E * 8), (e0 + tid * 2) * 8, 0, AUX_SC1 | 1);
        }
    }
    asm volatile(".Lke%c0:" :: "i"((int)FULL));
}

__global__ void __launch_bounds__(256)
gemnet_scatter_kernel(const u16* __restrict__ m, const int* __restrict__ idx_in, const int* __restrict__ idx_out,
                      const u16* __restrict__ angle, const u16* __restrict__ ang_freq, float* acc, int T, int De, int L)
{
    const long long g = (long long)blockIdx.x * 256 + threadIdx.x;
    const int t = (int)(g / De), k = (int)(g - (long long)t * De);
    if (t >= T) return;
    const float mv = bf2f(m[(size_t)idx_in[t] * De + k]), a = bf2f(angle[t]);
    for (int l = 0; l < L; l++)
        atomicAdd(&acc[((size_t)idx_out[t] * L + l) * De + k], rbf16(cosf(rbf16(bf2f(ang_freq[l]) * a))) * mv);
}
__global__ void __launch_bounds__(256)
gemnet_edge_kernel(const u16* __restrict__ m, const u16* __restrict__ dist, const u16* __restrict__ centers,
                   const u16* __restrict__ w_rbf, const u16* __restrict__ w_sbf, const u16* __restrict__ w_down,
                   const u16* __restrict__ w_up, const float* __restrict__ acc, u16* __restrict__ out, int E, int De, int NR, int L)
{
    const long long g = (long long)blockIdx.x * 256 + threadIdx.x;
    const int e = (int)(g / De), k = (int)(g - (long long)e * De);
    if (e >= E) return;
    const float d = bf2f(dist[e]);
    auto rbf = [&](int r) { const float df = rbf16(d - bf2f(centers[r])); return rbf16(expf(rbf16(-10.0f * rbf16(df * df)))); };
    float hk = 0.f, up = 0.f;
    for (int r = 0; r < NR; r++) hk += rbf(r) * bf2f(w_rbf[r * De + k]);
    for (int j = 0; j < De; j++) {
        float agg = 0.f;
        for (int l = 0; l < L; l++) {
            float gl = 0.f, v = 0.f;
            for (int r = 0; r < NR; r++) gl += rbf(r) * bf2f(w_sbf[(r * L + l) * De + j]);
            for (int i = 0; i < De; i++) v += acc[((size_t)e * L + l) * De + i] * bf2f(w_down[i * De + j]);
            agg += v * gl;
        }
        up += rbf16(agg) * bf2f(w_up[j * De + k]);
    }
    const float o = bf2f(m[(size_t)e * De + k]) + rbf16(rbf16(up) * rbf16(hk));
    out[(size_t)e * De + k] = (u16)pk_bf16(o, o);
}

extern "C" void kernel_launch(void* const* d_in, const int* in_sizes, int n_in,
                              void* d_out, int out_size, void* d_ws, size_t ws_size,
                              hipStream_t stream) {
    const int E = in_sizes[3], T = in_sizes[1], NR = in_sizes[5], L = in_sizes[6];
    const int De = in_sizes[0] / E;
    const u32 W = (((u32)E + 31) / 32 + 15) & ~15u;
    const u32 WI = (((u32)E + NH * NS - 1) / (NH * NS) + 15) & ~15u;
    const u64 magW = (0x100000000ull + W - 1) / W, magI = (0x100000000ull + WI - 1) / WI;
    const int T4 = T / 4;
    const int RF = T4 / (NWG * NT);
    const int RQ = (T4 - RF * NWG * NT + NWG - 1) / NWG;
    int dev = 0, cus = 0;
    (void)hipGetDevice(&dev);
    (void)hipDeviceGetAttribute(&cus, hipDeviceAttributeMultiprocessorCount, dev);
    const bool fused = De == 4 && NR == 8 && L == 1 && E % 2 == 0 && T % 4 == 0 && RF < NQ && W <= (u32)MAXW && WI <= (u32)MAXWI &&
                       magW < (1u << 24) && magI < (1u << 24) &&
                       (u64)E * (magW * W - 0x100000000ull) < 0x100000000ull && (u64)E * (magI * WI - 0x100000000ull) < 0x100000000ull &&
                       ws_size >= WS_END && cus >= NWG;
    if (fused) {
        const float rfac = 1.2f * (7.0f + 4.5f * sqrtf((float)T / ((float)E * NH))) / (float)E;
        auto kf = RF == NQ - 1 ? gemnet_fused_kernel<true> : gemnet_fused_kernel<false>;
        (void)hipFuncSetAttribute((const void*)kf, hipFuncAttributeMaxDynamicSharedMemorySize, LDS_BYTES);
        kf<<<NWG, NT, LDS_BYTES, stream>>>((const u32x4*)d_in[1], (const u32x4*)d_in[2], (const u32x2*)d_in[4],
            (const u16*)d_in[6], (const u32x4*)d_in[0], (char*)d_ws, (const u16*)d_in[3],
            (const u32*)d_in[5], (const u32*)d_in[7], (const u32*)d_in[8], (const u32*)d_in[9], (const u32*)d_in[10],
            (u32x2*)d_out, T4, RF, RQ, E, W, (u32)magW, WI, (u32)magI, rfac);
    } else {
        (void)hipMemsetAsync(d_ws, 0, (size_t)E * L * De * 4, stream);
        gemnet_scatter_kernel<<<(unsigned)(((long long)T * De + 255) / 256), 256, 0, stream>>>((const u16*)d_in[0], (const int*)d_in[1],
            (const int*)d_in[2], (const u16*)d_in[4], (const u16*)d_in[6], (float*)d_ws, T, De, L);
        gemnet_edge_kernel<<<(unsigned)(((long long)E * De + 255) / 256), 256, 0, stream>>>((const u16*)d_in[0], (const u16*)d_in[3],
            (const u16*)d_in[5], (const u16*)d_in[7], (const u16*)d_in[8], (const u16*)d_in[9], (const u16*)d_in[10],
            (const float*)d_ws, (u16*)d_out, E, De, NR, L);
    }
}
```
